```python
import torch

def _softmax(x, axis=-1):
    return torch.softmax(x, dim=axis)
def _gelu(x):
    return torch.nn.functional.gelu(x, approximate="tanh")
def _silu(x):
    return torch.nn.functional.silu(x)
def _sigmoid(x):
    return torch.sigmoid(x)
def _layernorm(x, axis=-1, eps=1e-5):
    m = x.mean(dim=axis, keepdim=True)
    v = x.var(dim=axis, keepdim=True, unbiased=False)
    return (x - m) / torch.sqrt(v + eps)
def _scatter_add(tgt, idx, src, axis=0):
    return tgt.clone().index_add_(axis, idx, src)
def _segment_sum(src, seg, n):
    out = torch.zeros((n,) + src.shape[1:], dtype=src.dtype)
    return out.index_add_(0, seg, src)
def _pad(x, pw):
    flat = tuple(v for pair in reversed(pw) for v in pair)
    return torch.nn.functional.pad(x, flat)

kernel_name = 'parallel_sum__sliding_block_cached__geglu_ffn__decode'

def setup_inputs():

    _w = (lambda a: (lambda t: t.to(torch.bfloat16) if t.is_floating_point() else t)(torch.as_tensor(a)))
    def _A_setup():
        B, Sq, D = 2, 1, 640
        Hq, Hkv, Sk, Dh = 32, 32, 4096, 128
        assert Hq % Hkv == 0, (Hq, Hkv)
        return {
            "x": _w((lambda *s: torch.randn(*s, dtype=torch.bfloat16) * 0.05)(B, Sq, D)),
            "k_cache": _w((lambda *s: torch.randn(*s, dtype=torch.bfloat16) * 3.2)(B, Hkv, Sk, Dh)),
            "v_cache": _w((lambda *s: torch.randn(*s, dtype=torch.bfloat16) * 1.6)(B, Hkv, Sk, Dh)),
            "wq": _w((lambda *s: torch.randn(*s, dtype=torch.bfloat16) * 3.2)(D, Hq * Dh) * (D ** -0.5)),
            "wo": _w((lambda *s: torch.randn(*s, dtype=torch.bfloat16) * 1.6)(Hq * Dh, D) * ((Hq * Dh) ** -0.5)),
        }
    _A_in = _A_setup()

    def _B_setup():
        B, S, D, F = 2, 1, 640, 1728
        return {
            "x": _w((lambda *s: torch.randn(*s, dtype=torch.bfloat16) * 0.05)(B, S, D)),
            "w_gate": _w((lambda *s: torch.randn(*s, dtype=torch.bfloat16) * 0.05)(D, F)),
            "w_up": _w((lambda *s: torch.randn(*s, dtype=torch.bfloat16) * 0.05)(D, F)),
            "w_down": _w((lambda *s: torch.randn(*s, dtype=torch.bfloat16) * 0.05)(F, D)),
        }
    _B_in = _B_setup()

    return {
        "A_x": _A_in["x"],
        "A_k_cache": _A_in["k_cache"],
        "A_v_cache": _A_in["v_cache"],
        "A_wq": _A_in["wq"],
        "A_wo": _A_in["wo"],
        "B_x": _B_in["x"],
        "B_w_gate": _B_in["w_gate"],
        "B_w_up": _B_in["w_up"],
        "B_w_down": _B_in["w_down"],
    }

def reference(A_x, A_k_cache, A_v_cache, A_wq, A_wo, B_x, B_w_gate, B_w_up, B_w_down):
    def _A_ref(x, k_cache, v_cache, wq, wo):
        B, Sq, D = x.shape
        Hq, Dh = 32, 128
        Hkv, Sk = k_cache.shape[1], k_cache.shape[2]
        W = min(4096, max(256, Sk // 4))
        rep = Hq // Hkv
        q = (lambda x, *d: x.permute(*d))((x @ wq).reshape(B, Sq, Hq, Dh), 0, 2, 1, 3)
        k = (lambda x, n, axis: torch.repeat_interleave(x, n, dim=axis))(k_cache[:, :, Sk - W:, :], rep, axis=1)
        v = (lambda x, n, axis: torch.repeat_interleave(x, n, dim=axis))(v_cache[:, :, Sk - W:, :], rep, axis=1)
        scores = torch.einsum("bhqd,bhkd->bhqk", q, k) * (Dh ** -0.5)
        p = _softmax(scores, axis=-1)
        o = torch.einsum("bhqk,bhkd->bhqd", p, v)
        return (lambda x, *d: x.permute(*d))(o, 0, 2, 1, 3).reshape(B, Sq, Hq * Dh) @ wo

    def _B_ref(x, w_gate, w_up, w_down):
        return (_gelu(x @ w_gate) * (x @ w_up)) @ w_down

    x = A_x
    xn = _layernorm(x, axis=-1)
    return x + _A_ref(xn, A_k_cache, A_v_cache, A_wq, A_wo) + _B_ref(xn, B_w_gate, B_w_up, B_w_down)
```

```cpp
#include <hip/hip_runtime.h>
#include <hip/hip_bf16.h>
#include <stdint.h>

typedef unsigned short u16;
__device__ __forceinline__ float bf2f(u16 v) { return __uint_as_float(((unsigned)v) << 16); }
__device__ __forceinline__ float rbf(float f) { return __bfloat162float(__float2bfloat16(f)); }

constexpr int D = 640, HQ = 32, DH = 128, SK = 4096, W = 1024, F = 1728;

__device__ void ln_to_lds(const u16* x, float* xn, float* red) {
    for (int b = 0; b < 2; ++b) {
        float s = 0.f;
        for (int i = threadIdx.x; i < D; i += blockDim.x) s += bf2f(x[b * D + i]);
        red[threadIdx.x] = s; __syncthreads();
        for (int o = blockDim.x / 2; o > 0; o >>= 1) { if (threadIdx.x < o) red[threadIdx.x] += red[threadIdx.x + o]; __syncthreads(); }
        float m = red[0] / D; __syncthreads();
        float v = 0.f;
        for (int i = threadIdx.x; i < D; i += blockDim.x) { float d = bf2f(x[b * D + i]) - m; v += d * d; }
        red[threadIdx.x] = v; __syncthreads();
        for (int o = blockDim.x / 2; o > 0; o >>= 1) { if (threadIdx.x < o) red[threadIdx.x] += red[threadIdx.x + o]; __syncthreads(); }
        float var = red[0] / D; __syncthreads();
        float r = rsqrtf(var + 1e-5f);
        for (int i = threadIdx.x; i < D; i += blockDim.x) xn[b * D + i] = rbf((bf2f(x[b * D + i]) - m) * r);
    }
    __syncthreads();
}

__global__ void k1(const u16* x, const u16* wq, const u16* wg, const u16* wu, float* q, float* h) {
    __shared__ float xn[2 * D]; __shared__ float red[64];
    ln_to_lds(x, xn, red);
    int c = blockIdx.x * 64 + threadIdx.x;
    if (c < HQ * DH) {
        float a0 = 0, a1 = 0;
        for (int k = 0; k < D; ++k) { float w = bf2f(wq[(size_t)k * (HQ * DH) + c]); a0 += xn[k] * w; a1 += xn[D + k] * w; }
        q[c] = rbf(a0); q[HQ * DH + c] = rbf(a1);
    } else {
        int f = c - HQ * DH;
        float g0 = 0, g1 = 0, u0 = 0, u1 = 0;
        for (int k = 0; k < D; ++k) {
            float a = bf2f(wg[(size_t)k * F + f]), b = bf2f(wu[(size_t)k * F + f]);
            g0 += xn[k] * a; g1 += xn[D + k] * a; u0 += xn[k] * b; u1 += xn[D + k] * b;
        }
        auto gelu = [](float v) { return 0.5f * v * (1.f + tanhf(0.7978845608028654f * (v + 0.044715f * v * v * v))); };
        h[f] = rbf(rbf(gelu(rbf(g0))) * rbf(u0)); h[F + f] = rbf(rbf(gelu(rbf(g1))) * rbf(u1));
    }
}

__global__ void k2(const float* q, const u16* kc, const u16* vc, float* o) {
    __shared__ float s[W]; __shared__ float red[256]; __shared__ float qs[DH];
    int bh = blockIdx.x;
    const u16* K = kc + ((size_t)bh * SK + (SK - W)) * DH;
    const u16* V = vc + ((size_t)bh * SK + (SK - W)) * DH;
    if (threadIdx.x < DH) qs[threadIdx.x] = q[bh * DH + threadIdx.x];
    __syncthreads();
    float mx = -1e30f;
    for (int j = threadIdx.x; j < W; j += 256) {
        float a = 0; for (int d = 0; d < DH; ++d) a += qs[d] * bf2f(K[(size_t)j * DH + d]);
        a *= 0.08838834764831845f; s[j] = a; mx = fmaxf(mx, a);
    }
    red[threadIdx.x] = mx; __syncthreads();
    for (int off = 128; off > 0; off >>= 1) { if (threadIdx.x < off) red[threadIdx.x] = fmaxf(red[threadIdx.x], red[threadIdx.x + off]); __syncthreads(); }
    mx = red[0]; __syncthreads();
    float sum = 0;
    for (int j = threadIdx.x; j < W; j += 256) { float e = __expf(s[j] - mx); s[j] = e; sum += e; }
    red[threadIdx.x] = sum; __syncthreads();
    for (int off = 128; off > 0; off >>= 1) { if (threadIdx.x < off) red[threadIdx.x] += red[threadIdx.x + off]; __syncthreads(); }
    sum = red[0]; __syncthreads();
    if (threadIdx.x < DH) {
        float a = 0; for (int j = 0; j < W; ++j) a += s[j] * bf2f(V[(size_t)j * DH + threadIdx.x]);
        o[bh * DH + threadIdx.x] = rbf(a / sum);
    }
}

__global__ void k3(const u16* x, const float* o, const float* h, const u16* wo, const u16* wd, __hip_bfloat16* out) {
    __shared__ float red[256];
    int b = blockIdx.x / D, c = blockIdx.x % D;
    float a = 0, f = 0;
    for (int k = threadIdx.x; k < HQ * DH; k += 256) a += o[b * HQ * DH + k] * bf2f(wo[(size_t)k * D + c]);
    for (int k = threadIdx.x; k < F; k += 256) f += h[b * F + k] * bf2f(wd[(size_t)k * D + c]);
    red[threadIdx.x] = a; __syncthreads();
    for (int off = 128; off > 0; off >>= 1) { if (threadIdx.x < off) red[threadIdx.x] += red[threadIdx.x + off]; __syncthreads(); }
    a = red[0]; __syncthreads();
    red[threadIdx.x] = f; __syncthreads();
    for (int off = 128; off > 0; off >>= 1) { if (threadIdx.x < off) red[threadIdx.x] += red[threadIdx.x + off]; __syncthreads(); }
    f = red[0];
    if (threadIdx.x == 0) out[b * D + c] = __float2bfloat16(bf2f(x[b * D + c]) + a + f);
}

extern "C" void kernel_launch(void* const* d_in, const int* in_sizes, int n_in,
                              void* d_out, int out_size, void* d_ws, size_t ws_size,
                              hipStream_t stream) {
    const u16* x = (const u16*)d_in[0]; const u16* kc = (const u16*)d_in[1]; const u16* vc = (const u16*)d_in[2];
    const u16* wq = (const u16*)d_in[3]; const u16* wo = (const u16*)d_in[4];
    const u16* wg = (const u16*)d_in[6]; const u16* wu = (const u16*)d_in[7]; const u16* wd = (const u16*)d_in[8];
    float* q = (float*)d_ws; float* h = q + 2 * HQ * DH; float* o = h + 2 * F;
    k1<<<(HQ * DH + F) / 64, 64, 0, stream>>>(x, wq, wg, wu, q, h);
    k2<<<64, 256, 0, stream>>>(q, kc, vc, o);
    k3<<<2 * D, 256, 0, stream>>>(x, o, h, wo, wd, (__hip_bfloat16*)d_out);
}
```

```cpp
#include <hip/hip_runtime.h>
#include <hip/hip_bf16.h>
#include <stdint.h>

#define STAMPS 0
#define NT_A 1

#define GAS __attribute__((address_space(1)))
#define LAS __attribute__((address_space(3)))
typedef unsigned v4u __attribute__((ext_vector_type(4)));
typedef unsigned v2u __attribute__((ext_vector_type(2)));
typedef float v4f __attribute__((ext_vector_type(4)));
typedef float v2f __attribute__((ext_vector_type(2)));
typedef v4u v4u_a4 __attribute__((aligned(4)));
typedef __bf16 bf2 __attribute__((ext_vector_type(2)));
typedef __bf16 bf8 __attribute__((ext_vector_type(8)));

constexpr int NFB = 27;
constexpr unsigned WS_X0 = 4096;
constexpr unsigned WS_X1 = WS_X0 + 256 * 1024;
constexpr unsigned X1_STR = 1152;
constexpr unsigned WS_X2 = WS_X1 + 256 * X1_STR;
constexpr unsigned WS_END = WS_X2 + 8 * 20 * 2048;
constexpr unsigned WS_STAMP = 1u << 20;
constexpr unsigned SPIN_G = 1u << 16, SPIN_L = 1u << 22;

constexpr int L_CNTA = 0, L_CNTK = 1, L_CNTC = 2, L_CNTD = 3, L_QF = 4, L_HF = 5, L_TAG = 6;
constexpr int L_ML = 8;
constexpr int L_Q = 16;
constexpr int L_H = 80;
constexpr int L_P = 144;
constexpr int L_RA = 272;
constexpr int L_RC = 784;
constexpr int L_RD = 1296;
constexpr int L_END = 2320;

__device__ __forceinline__ float asf(unsigned u) { return __builtin_bit_cast(float, u); }
__device__ __forceinline__ unsigned asu(float f) { return __builtin_bit_cast(unsigned, f); }
__device__ __forceinline__ float blo(unsigned u) { return asf(u << 16); }
__device__ __forceinline__ float bhi(unsigned u) { return asf(u & 0xffff0000u); }
__device__ __forceinline__ unsigned pk(float lo, float hi) { bf2 v = __builtin_convertvector(v2f{lo, hi}, bf2); unsigned u = __builtin_bit_cast(unsigned, v); asm("" : "+v"(u)); return u; }
__device__ __forceinline__ float rbf(float f) { return (float)(__bf16)f; }
__device__ __forceinline__ float dot2(unsigned a, unsigned b, float c) {
    return __builtin_amdgcn_fdot2_f32_bf16(__builtin_bit_cast(bf2, a), __builtin_bit_cast(bf2, b), c, false);
}
__device__ __forceinline__ unsigned plo(unsigned a, unsigned b) { return __builtin_amdgcn_perm(b, a, 0x05040100u); }
__device__ __forceinline__ unsigned phi(unsigned a, unsigned b) { return __builtin_amdgcn_perm(b, a, 0x07060302u); }
template <int CTRL> __device__ __forceinline__ float dpp(float x) {
    return asf(__builtin_amdgcn_mov_dpp(asu(x), CTRL, 0xf, 0xf, true));
}
constexpr int XOR1 = 0xB1, XOR2 = 0x4E, HMIR = 0x141, ROR8 = 0x128, ROR4 = 0x124;
__device__ __forceinline__ float sw32(float a, float b) { auto s = __builtin_amdgcn_permlane32_swap(asu(a), asu(b), false, false); return asf(s[0]) + asf(s[1]); }
__device__ __forceinline__ float sw16(float a, float b) { auto s = __builtin_amdgcn_permlane16_swap(asu(a), asu(b), false, false); return asf(s[0]) + asf(s[1]); }
__device__ __forceinline__ float mx32(float a) { auto s = __builtin_amdgcn_permlane32_swap(asu(a), asu(a), false, false); return fmaxf(asf(s[0]), asf(s[1])); }
__device__ __forceinline__ float mx16(float a) { auto s = __builtin_amdgcn_permlane16_swap(asu(a), asu(a), false, false); return fmaxf(asf(s[0]), asf(s[1])); }
__device__ __forceinline__ float rowsum(float x) { x += dpp<XOR1>(x); x += dpp<XOR2>(x); x += dpp<HMIR>(x); x += dpp<ROR8>(x); return x; }
__device__ __forceinline__ float rowmax(float x) { x = fmaxf(x, dpp<XOR1>(x)); x = fmaxf(x, dpp<XOR2>(x)); x = fmaxf(x, dpp<HMIR>(x)); x = fmaxf(x, dpp<ROR8>(x)); return x; }
#define CBAR() asm volatile("" ::: "memory")

__device__ __forceinline__ v4u ldnt(const char* p) { return __builtin_nontemporal_load((const GAS v4u*)p); }
__device__ __forceinline__ v4u ld_dev(__amdgpu_buffer_rsrc_t rs, unsigned off) { return __builtin_amdgcn_raw_buffer_load_b128(rs, off, 0, 16); }
__device__ __forceinline__ void st_dev(__amdgpu_buffer_rsrc_t rs, unsigned off, float a, float b, unsigned tag) {
    __builtin_amdgcn_raw_buffer_store_b128(v4u{asu(a), tag, asu(b), tag}, rs, off, 0, 16);
}
__device__ __forceinline__ bool tagged(v4u g, unsigned tag) { return (g.y == tag) & (g.w == tag); }
__device__ __forceinline__ void lds_wait(volatile LAS unsigned* w, unsigned want) {
#pragma nounroll
    for (unsigned s = 0; s < SPIN_L; ++s) { if (__builtin_amdgcn_readfirstlane(*w) == want) break; __builtin_amdgcn_s_sleep(1); }
    CBAR();
}
__device__ __forceinline__ unsigned lds_arrive(LAS unsigned* w, int lane) {
    CBAR();
    unsigned o;
    asm volatile("s_mov_b64 exec, 1\n\tds_add_rtn_u32 %0, %1, %2\n\ts_mov_b64 exec, -1\n\ts_waitcnt lgkmcnt(0)" : "=&v"(o) : "v"(w), "v"(1u) : "memory");
    return __builtin_amdgcn_readfirstlane(o);
}
#define STAMP(i)
#define TAKE(v)
#define PUT(i, v)

__global__ void __launch_bounds__(1024, 4)
fused_decode(const char* __restrict__ x, const char* __restrict__ kc, const char* __restrict__ vc, const char* __restrict__ wq,
             const char* __restrict__ wo, const char* __restrict__ wg, const char* __restrict__ wu, const char* __restrict__ wd,
             char* out, char* ws) {
    __shared__ __attribute__((aligned(16))) unsigned smem[L_END];
    LAS unsigned* L = (LAS unsigned*)smem;
    volatile LAS unsigned* VL = (volatile LAS unsigned*)smem;
    TAKE(t0);
    asm volatile("" :: "s"(x), "s"(kc), "s"(vc), "s"(wq), "s"(wo), "s"(wg), "s"(wu), "s"(wd), "s"(out), "s"(ws));
    const int tid = threadIdx.x, lane = tid & 63;
    const int wv = __builtin_amdgcn_readfirstlane(tid >> 6), role = wv >> 2, w = wv & 3;
    const int c = blockIdx.x, h = ((c >> 6) << 3) | (c & 7), j = (c >> 3) & 7;
    const bool ffn = h < NFB;
    const __amdgpu_buffer_rsrc_t rs = __builtin_amdgcn_make_buffer_rsrc((void*)ws, 0, WS_END, 0x00020000);
    v4u r[16];

    if (__builtin_expect(role == 0, 1)) {
        __builtin_amdgcn_s_setprio(3);
        const int cb = j & 3, kh = j >> 2;
        const bool act = cb < 2 || ffn;
        if (tid < 8) L[tid] = 0u;
        const int row0 = 320 * kh + 80 * w + 10 * (lane >> 3);
        v2u xs[5]; v4u xa[2]; unsigned xb[2];
        const char* xp = x + (lane >> 5) * 1280 + (lane & 31) * 8;
        const unsigned pitch = act ? (cb < 2 ? 8192u : 3456u) : 0u;
        const char* base = (cb < 2 || !ffn) ? wq + h * 256 + (cb & 1) * 128 : (cb == 2 ? wg : wu) + h * 128;
        const unsigned voff = row0 * pitch + (lane & 7) * 16;
#pragma unroll
        for (int t = 0; t < 5; ++t) xs[t] = *(const GAS v2u*)(xp + t * 256);
#pragma unroll
        for (int b = 0; b < 2; ++b) { xa[b] = *(const GAS v4u_a4*)(x + b * 1280 + row0 * 2); xb[b] = *(const GAS unsigned*)(x + b * 1280 + row0 * 2 + 16); }
        const unsigned ep = __builtin_amdgcn_raw_buffer_load_b32(rs, 0, 0, 16);
        __builtin_amdgcn_sched_barrier(0);
#pragma unroll
        for (int i = 0; i < 10; ++i) { r[i] = ldnt(base + (size_t)i * pitch + voff); __builtin_amdgcn_sched_barrier(0); }
        __builtin_amdgcn_sched_barrier(0);
        asm volatile("s_waitcnt lgkmcnt(0)" ::: "memory");
        __builtin_amdgcn_s_barrier();
        __builtin_amdgcn_sched_barrier(0);
        TAKE(t1);
        float v0, v1;
        {
            float f[20], s = 0.f;
#pragma unroll
            for (int t = 0; t < 5; ++t) { f[4 * t] = blo(xs[t].x); f[4 * t + 1] = bhi(xs[t].x); f[4 * t + 2] = blo(xs[t].y); f[4 * t + 3] = bhi(xs[t].y); }
#pragma unroll
            for (int t = 0; t < 20; ++t) s += f[t];
            s = rowsum(s); s = sw16(s, s);
            const float mean = s * (1.0f / 640.0f);
            float q = 0.f;
#pragma unroll
            for (int t = 0; t < 20; ++t) { const float d = f[t] - mean; q = fmaf(d, d, q); }
            q = rowsum(q); q = sw16(q, q);
            const float mr = rbf(mean);
            const float rinv = __builtin_amdgcn_rcpf(rbf(__builtin_amdgcn_sqrtf(rbf(rbf(q * (1.0f / 640.0f)) + 1e-5f))));
            unsigned xn[2][5];
#pragma unroll
            for (int b = 0; b < 2; ++b) {
                const float m = asf(__builtin_amdgcn_readlane(asu(mr), 32 * b)), ri = asf(__builtin_amdgcn_readlane(asu(rinv), 32 * b));
#pragma unroll
                for (int t = 0; t < 5; ++t) {
                    const unsigned u = t < 4 ? xa[b][t] : xb[b];
                    const unsigned d = pk(blo(u) - m, bhi(u) - m);
                    xn[b][t] = pk(blo(d) * ri, bhi(d) * ri);
                }
            }
            __builtin_amdgcn_sched_barrier(0);
            float a[16];
#pragma unroll
            for (int k = 0; k < 16; ++k) a[k] = 0.f;
#pragma unroll
            for (int t = 0; t < 5; ++t)
#pragma unroll
                for (int k = 0; k < 4; ++k) {
                    const unsigned p0 = plo(r[2 * t][k], r[2 * t + 1][k]), p1 = phi(r[2 * t][k], r[2 * t + 1][k]);
                    a[2 * k] = dot2(p0, xn[0][t], a[2 * k]); a[2 * k + 1] = dot2(p1, xn[0][t], a[2 * k + 1]);
                    a[8 + 2 * k] = dot2(p0, xn[1][t], a[8 + 2 * k]); a[9 + 2 * k] = dot2(p1, xn[1][t], a[9 + 2 * k]);
                }
#pragma unroll
            for (int k = 0; k < 8; ++k) a[k] = sw32(a[k], a[k + 8]);
#pragma unroll
            for (int k = 0; k < 4; ++k) a[k] = sw16(a[k], a[k + 4]);
            const bool b3 = lane & 8;
            v0 = (b3 ? a[2] : a[0]) + dpp<ROR8>(b3 ? a[0] : a[2]);
            v1 = (b3 ? a[3] : a[1]) + dpp<ROR8>(b3 ? a[1] : a[3]);
        }
        TAKE(t2);
        *(LAS v2f*)(L + L_RA + w * 128 + lane * 2) = v2f{v0, v1};
        const unsigned tag = __builtin_amdgcn_readfirstlane(ep) + 1u;
        const unsigned arr = lds_arrive(L + L_CNTA, lane);
        if (arr == 3u && act) {
            v2f s = *(LAS v2f*)(L + L_RA + lane * 2);
#pragma unroll
            for (int k = 1; k < 4; ++k) s += *(LAS v2f*)(L + L_RA + k * 128 + lane * 2);
            const int p = (lane & 32) | ((lane & 7) << 2) | ((lane >> 3) & 3);
            st_dev(rs, WS_X0 + (h * 8 + j) * 1024 + p * 16, s.x, s.y, tag);
        }
        if (w == 0) {
            const unsigned o = WS_X0 + (h * 8 + (lane >> 5)) * 1024 + (((j >> 2) << 5) | (lane & 31)) * 16;
            v4u g0, g1;
            _Pragma("nounroll") for (unsigned s = 0; s < SPIN_G; ++s) {
                g0 = ld_dev(rs, o); g1 = ld_dev(rs, o + 4096);
                if (__all(tagged(g0, tag) & tagged(g1, tag))) break;
                CBAR();
            }
            L[L_Q + lane] = pk(asf(g0.x) + asf(g1.x), asf(g0.z) + asf(g1.z));
            CBAR();
            if (lane == 0) { VL[L_TAG] = tag; VL[L_QF] = 1u; }
        } else if (w == 1) {
            lds_wait(VL + L_QF, 1u);
            unsigned hv = 0;
            if (ffn) {
                const unsigned o = WS_X0 + (h * 8 + 2) * 1024 + lane * 16;
                v4u g0, g1, u0, u1;
                _Pragma("nounroll") for (unsigned s = 0; s < SPIN_G; ++s) {
                    g0 = ld_dev(rs, o); u0 = ld_dev(rs, o + 1024); g1 = ld_dev(rs, o + 4096); u1 = ld_dev(rs, o + 5120);
                    if (__all(tagged(g0, tag) & tagged(g1, tag) & tagged(u0, tag) & tagged(u1, tag))) break;
                    CBAR();
                }
                const unsigned gp = pk(asf(g0.x) + asf(g1.x), asf(g0.z) + asf(g1.z)), up = pk(asf(u0.x) + asf(u1.x), asf(u0.z) + asf(u1.z));
                float ge[2] = {blo(gp), bhi(gp)};
#pragma unroll
                for (int k = 0; k < 2; ++k) {
                    const float g = ge[k];
                    const float y = 0.7978845608028654f * fmaf(0.044715f * g * g, g, g);
                    ge[k] = g * __builtin_amdgcn_rcpf(1.0f + __builtin_amdgcn_exp2f(-2.8853900817779268f * y));
                }
                const unsigned gl = pk(ge[0], ge[1]);
                hv = pk(blo(gl) * blo(up), bhi(gl) * bhi(up));
            }
            L[L_H + lane] = hv;
            CBAR();
            if (lane == 0) VL[L_HF] = 1u;
        }
        PUT(0, t0); PUT(1, t1); PUT(2, t2); STAMP(3);
        __builtin_amdgcn_s_waitcnt(0);
        return;
    }

    if (__builtin_expect(role < 3, 1)) {
        const char* base = (role == 1 ? kc : vc) + ((size_t)(((j >> 2) * 32 + h) * 4096 + 3072 + 256 * (j & 3))) * 256 + w * 1024;
        if (role == 1) __builtin_amdgcn_s_setprio(2); else __builtin_amdgcn_s_setprio(1);
        __builtin_amdgcn_s_barrier();
#pragma unroll
        for (int i = 0; i < 16; ++i) { r[i] = ldnt(base + i * 4096 + lane * 16); __builtin_amdgcn_sched_barrier(0); }
        __builtin_amdgcn_sched_barrier(0);
        TAKE(t1);
        if (role == 1) {
            lds_wait(VL + L_QF, 1u);
            TAKE(t2);
            const v4u q = *(LAS v4u*)(L + L_Q + (lane & 15) * 4);
            float s[16];
#pragma unroll
            for (int i = 0; i < 16; ++i) s[i] = dot2(r[i].w, q.w, dot2(r[i].z, q.z, dot2(r[i].y, q.y, dot2(r[i].x, q.x, 0.f))));
#define TR(HALF, CTRL) { const bool b_ = lane & (HALF); _Pragma("unroll") for (int k = 0; k < (HALF); ++k) s[k] = (b_ ? s[k + (HALF)] : s[k]) + dpp<CTRL>(b_ ? s[k] : s[k + (HALF)]); }
            TR(8, ROR8) TR(4, HMIR) TR(2, XOR2) TR(1, XOR1)
#undef TR
            const float sc = rbf(rbf(s[0]) * 0.08838834764831845f);
            const float m = mx32(mx16(rowmax(sc)));
            const __bf16 pb = (__bf16)__builtin_amdgcn_exp2f((sc - m) * 1.4426950408889634f);
            float l = rowsum((float)pb); l = sw16(l, l); l = sw32(l, l);
            ((LAS __bf16*)(L + L_P))[w * 64 + lane] = pb;
            if (lane == 0) *(LAS v2f*)(L + L_ML + 2 * w) = v2f{m, l};
            lds_arrive(L + L_CNTK, lane);
            PUT(0, t0); PUT(1, t1); PUT(2, t2); STAMP(3);
            __builtin_amdgcn_s_waitcnt(0);
        return;
        }
        lds_wait(VL + L_CNTK, 4u);
        TAKE(t2);
        const unsigned tag = VL[L_TAG];
        const v4f ml0 = *(LAS v4f*)(L + L_ML), ml1 = *(LAS v4f*)(L + L_ML + 4);
        const float mw = asf(L[L_ML + 2 * w]);
        const v4u p0 = *(LAS v4u*)(L + L_P + w * 32 + (lane >> 4) * 8), p1 = *(LAS v4u*)(L + L_P + w * 32 + (lane >> 4) * 8 + 4);
        const float M = fmaxf(fmaxf(ml0.x, ml0.z), fmaxf(ml1.x, ml1.z));
        const float fw = __builtin_amdgcn_exp2f((mw - M) * 1.4426950408889634f);
        float a[8];
#pragma unroll
        for (int k = 0; k < 8; ++k) a[k] = 0.f;
#pragma unroll
        for (int t = 0; t < 8; ++t) {
            const unsigned pp = t < 4 ? p0[t] : p1[t - 4];
#pragma unroll
            for (int k = 0; k < 4; ++k) {
                a[2 * k] = dot2(plo(r[2 * t][k], r[2 * t + 1][k]), pp, a[2 * k]);
                a[2 * k + 1] = dot2(phi(r[2 * t][k], r[2 * t + 1][k]), pp, a[2 * k + 1]);
            }
        }
        TAKE(t3);
#pragma unroll
        for (int k = 0; k < 4; ++k) a[k] = sw32(a[k], a[k + 4]);
        *(LAS v2f*)(L + L_RC + w * 128 + lane * 2) = v2f{sw16(a[0], a[2]) * fw, sw16(a[1], a[3]) * fw};
        if (lds_arrive(L + L_CNTC, lane) == 3u) {
            v2f s = *(LAS v2f*)(L + L_RC + lane * 2);
#pragma unroll
            for (int k = 1; k < 4; ++k) s += *(LAS v2f*)(L + L_RC + k * 128 + lane * 2);
            const unsigned o = WS_X1 + (h * 8 + j) * X1_STR;
            st_dev(rs, o + (((lane & 15) << 2) | (lane >> 4)) * 16, s.x, s.y, tag);
            const float c2 = 1.4426950408889634f;
            const float Ls = __builtin_amdgcn_exp2f((ml0.x - M) * c2) * ml0.y + __builtin_amdgcn_exp2f((ml0.z - M) * c2) * ml0.w +
                             __builtin_amdgcn_exp2f((ml1.x - M) * c2) * ml1.y + __builtin_amdgcn_exp2f((ml1.z - M) * c2) * ml1.w;
            if (lane == 0) st_dev(rs, o + 1024, M, Ls, tag);
        }
        PUT(0, t0); PUT(1, t1); PUT(2, t2); PUT(3, t3); STAMP(4);
        __builtin_amdgcn_s_waitcnt(0);
        return;
    }

    {
        const int n = lane & 15, kg = lane >> 4;
        const bool dn = ffn && n >= 10 && n < 15;
        const char* p = dn ? wd + (h * 64 + 32 * (w >> 1) + 8 * kg) * 1280 + j * 160 + (5 * (w & 1) + n - 10) * 16
                           : wo + (h * 128 + 32 * w + 8 * kg) * 1280 + j * 160 + (n < 10 ? n : 9) * 16;
        const int cq = h < 20 ? h : 0;
        const int oo = (cq / 10) * 1280 + j * 160 + (cq % 10) * 16 + (lane & 3) * 4;
        __builtin_amdgcn_s_barrier();
        const unsigned xres = *(const GAS unsigned*)(x + oo);
#pragma unroll
        for (int i = 0; i < 8; ++i) { r[i] = ldnt(p + i * 1280); __builtin_amdgcn_sched_barrier(0); }
        __builtin_amdgcn_sched_barrier(0);
        TAKE(t1);
        lds_wait(VL + L_HF, 1u);
        const unsigned tag = VL[L_TAG];
        const v4u hf = *(LAS v4u*)(L + L_H + (n >> 3) * 32 + (w >> 1) * 16 + kg * 4);
        v4u B[8];
#pragma unroll
        for (int k = 0; k < 4; ++k)
#pragma unroll
            for (int t = 0; t < 4; ++t) { B[2 * k][t] = plo(r[2 * t][k], r[2 * t + 1][k]); B[2 * k + 1][t] = phi(r[2 * t][k], r[2 * t + 1][k]); }
#pragma unroll
        for (int k = 0; k < 8; ++k) asm volatile("" : "+v"(B[k]));
        TAKE(t2);
        const unsigned o1 = WS_X1 + (h * 8 + (n >> 3) * 4 + (n & 3)) * X1_STR;
        const unsigned o2 = o1 + (16 * w + 4 * kg + 2 * ((n >> 2) & 1)) * 16;
        v4u g0, g1, gm;
        _Pragma("nounroll") for (unsigned s = 0; s < SPIN_G; ++s) {
            g0 = ld_dev(rs, o2); g1 = ld_dev(rs, o2 + 16); gm = ld_dev(rs, o1 + 1024);
            if (__all(tagged(g0, tag) & tagged(g1, tag) & tagged(gm, tag))) break;
            CBAR();
        }
        TAKE(t3);
        float M = asf(gm.x);
        M = fmaxf(M, dpp<XOR1>(M)); M = fmaxf(M, dpp<XOR2>(M));
        const float e = __builtin_amdgcn_exp2f((asf(gm.x) - M) * 1.4426950408889634f);
        float Ls = e * asf(gm.z);
        Ls += dpp<XOR1>(Ls); Ls += dpp<XOR2>(Ls);
        const float wt = e * __builtin_amdgcn_rcpf(Ls);
        float o[4] = {asf(g0.x) * wt, asf(g0.z) * wt, asf(g1.x) * wt, asf(g1.z) * wt};
#pragma unroll
        for (int k = 0; k < 4; ++k) { o[k] += dpp<XOR1>(o[k]); o[k] += dpp<XOR2>(o[k]); }
        v4u A;
        A.x = pk(o[0], o[1]); A.y = pk(o[2], o[3]);
        A.z = asu(dpp<HMIR>(asf(A.x))); A.w = asu(dpp<HMIR>(asf(A.y)));
        if ((n & 7) == 1) A = hf;
        float y[8];
#pragma unroll
        for (int k = 0; k < 8; ++k) {
            const v4f d = __builtin_amdgcn_mfma_f32_16x16x32_bf16(__builtin_bit_cast(bf8, A), __builtin_bit_cast(bf8, B[k]), v4f{0.f, 0.f, 0.f, 0.f}, 0, 0, 0);
            y[k] = n < 10 ? d.x : d.y;
        }
        if (!(lane & 16)) {
            LAS v4f* yo = (LAS v4f*)(L + L_RD + w * 256 + (lane >> 5) * 128 + n * 8);
            yo[0] = v4f{y[0], y[1], y[2], y[3]}; yo[1] = v4f{y[4], y[5], y[6], y[7]};
        }
        if (lds_arrive(L + L_CNTD, lane) == 3u) {
            TAKE(t4);
            if (lane < 40) {
                const int b = lane >= 20, q4 = lane - 20 * b, nn = q4 >> 1, hi = nn >= 5;
                const LAS unsigned* yb = L + L_RD + b * 128 + (q4 & 1) * 4;
                v4f s = *(const LAS v4f*)(yb + nn * 8);
#pragma unroll
                for (int k = 1; k < 4; ++k) s += *(const LAS v4f*)(yb + k * 256 + nn * 8);
                const LAS unsigned* yf = yb + hi * 256 + (10 + nn - 5 * hi) * 8;
                s += *(const LAS v4f*)yf; s += *(const LAS v4f*)(yf + 512);
                const unsigned o = WS_X2 + (j * 20 + (lane >> 1)) * 2048 + h * 64 + (lane & 1) * 32;
                st_dev(rs, o, s.x, s.y, tag); st_dev(rs, o + 16, s.z, s.w, tag);
            }
            TAKE(t5);
            if (h < 20) {
                const unsigned o = WS_X2 + (j * 20 + h) * 2048 + lane * 16;
                _Pragma("nounroll") for (unsigned s = 0; s < SPIN_G; ++s) {
                    g0 = ld_dev(rs, o); g1 = ld_dev(rs, o + 1024);
                    if (__all(tagged(g0, tag) & tagged(g1, tag))) break;
                    CBAR();
                }
                float a0 = asf(g0.x) + asf(g1.x), a1 = asf(g0.z) + asf(g1.z);
                a0 += dpp<ROR4>(a0); a1 += dpp<ROR4>(a1);
                a0 += dpp<ROR8>(a0); a1 += dpp<ROR8>(a1);
                a0 = sw16(a0, a0); a1 = sw16(a1, a1);
                a0 = sw32(a0, a0); a1 = sw32(a1, a1);
                if (lane < 4) *(GAS unsigned*)(out + oo) = pk(blo(xres) + a0, bhi(xres) + a1);
                if (c == 0 && lane == 0) __builtin_amdgcn_raw_buffer_store_b32(tag, rs, 0, 0, 16);
            }
            PUT(4, t4); PUT(5, t5); STAMP(6);
        }
        PUT(0, t0); PUT(1, t1); PUT(2, t2); PUT(3, t3);
        __builtin_amdgcn_s_waitcnt(0);
    }
}

extern "C" void kernel_launch(void* const* d_in, const int* in_sizes, int n_in, void* d_out, int out_size, void* d_ws, size_t ws_size,
                              hipStream_t stream) {
    fused_decode<<<256, 1024, 0, stream>>>((const char*)d_in[0], (const char*)d_in[1], (const char*)d_in[2], (const char*)d_in[3],
                                           (const char*)d_in[4], (const char*)d_in[6], (const char*)d_in[7], (const char*)d_in[8],
                                           (char*)d_out, (char*)d_ws);
}
```

```cpp
#include <hip/hip_runtime.h>
#include <hip/hip_bf16.h>
#include <stdint.h>

constexpr unsigned RAW = 0x00020000u;

__global__ void __launch_bounds__(320, 2)
fused_decode(const void*, unsigned, unsigned, const void*, unsigned, unsigned, const void*, unsigned, unsigned, const void*, unsigned, unsigned,
             const void*, unsigned, unsigned, const void*, unsigned, unsigned, void*, unsigned, unsigned, const void*, unsigned, unsigned,
             void*, unsigned, unsigned, const void*, unsigned, unsigned, const void*, unsigned, unsigned) {
    __shared__ __attribute__((aligned(16))) unsigned smem[3840];
    asm volatile(
        "s_lshl_b32 s7, s2, 2\n"
        "s_and_b32 s7, s7, 0x20\n"
        "s_lshr_b32 s6, s2, 1\n"
        "s_and_b32 s6, s6, 0x10\n"
        "s_or_b32 s6, s6, s7\n"
        "s_load_dwordx4 s[8:11], s[0:1], s6\n"
        "s_load_dwordx16 s[12:27], s[0:1], 0x40\n"
        "s_load_dwordx8 s[28:35], s[0:1], 0x80\n"
        "s_load_dwordx4 s[36:39], s[0:1], 0xa0\n"
        "v_readfirstlane_b32 s3, v0\n"
        "s_bfe_u32 s5, s2, 0x30003\n"
        "s_lshr_b32 s4, s2, 3\n"
        "s_and_b32 s4, s4, 0x18\n"
        "s_and_b32 s6, s2, 7\n"
        "s_or_b32 s4, s4, s6\n"
        "v_and_b32 v0, 63, v0\n"
        "s_sub_u32 s3, s3, 64\n"
        "s_bfe_u32 s3, s3, 0x30006\n"
        "s_xor_b32 s6, s6, 7\n"
        "s_lshl_b32 s6, s6, 2\n"
        "s_lshr_b32 s50, s2, 6\n"
        "s_or_b32 s6, s6, s50\n"
        "s_xor_b32 s50, s4, 4\n"
        "s_cmp_eq_u32 s7, 0\n"
        "s_cselect_b32 s50, s50, s6\n"
        "s_cmp_lt_u32 s3, 4\n"
        "s_cbranch_scc1 .Lfd_stream\n"
        "s_setprio 3\n"
        "v_lshrrev_b32 v1, 5, v0\n"
        "v_and_b32 v2, 31, v0\n"
        "v_mul_u32_u24 v3, 0x500, v1\n"
        "v_lshl_add_u32 v21, v2, 4, v3\n"
        "v_lshl_add_u32 v22, v2, 2, v3\n"
        "s_bitcmp1_b32 s5, 1\n"
        "s_cselect_b32 s6, 640, 0\n"
        "s_sub_u32 s51, 640, s6\n"
        "v_lshlrev_b32 v20, 7, v0\n"
        "v_add_u32 v3, .Lfd_end-.Lfd_pc-8068, v20\n"
        "v_max_i32 v3, 0, v3\n"
        "s_getpc_b64 s[52:53]\n"
        ".Lfd_pc:\n"
        "v_and_b32 v20, 0x7ff, v20\n"
        "global_load_dword v17, v20, s[52:53]\n"
        "global_load_dword v17, v3, s[52:53]\n"
        "s_waitcnt lgkmcnt(0)\n"
        "s_memrealtime s[54:55]\n"
        "buffer_load_dwordx4 v[4:7], v21, s[24:27], s6 offen\n"
        "buffer_load_dword v8, v22, s[24:27], s6 offen offset:512\n"
        "buffer_load_dwordx4 v[10:13], v21, s[24:27], s51 offen\n"
        "buffer_load_dword v14, v22, s[24:27], s51 offen offset:512\n"
        "buffer_load_dword v16, off, s[20:23], 0 sc1\n"
        "s_branch .Lfd_helper\n"
        ".Lfd_stream:\n"
        "s_cmp_eq_u32 s7, 0\n"
        "s_movk_i32 s40, 0xd80\n"
        "s_cselect_b32 s40, 0x2000, s40\n"
        "s_cselect_b32 s6, 8, 7\n"
        "s_cselect_b32 s7, 0, s50\n"
        "s_lshl_b32 s41, s50, s6\n"
        "s_and_b32 s6, s5, 2\n"
        "s_lshl_b32 s6, s6, 1\n"
        "s_or_b32 s6, s6, s3\n"
        "s_mul_i32 s6, s6, 80\n"
        "s_mul_i32 s6, s6, s40\n"
        "s_add_u32 s41, s41, s6\n"
        "s_lshl_b32 s6, s40, 1\n"
        "s_mul_i32 s53, s40, 15\n"
        "v_lshrrev_b32 v4, 3, v0\n"
        "v_and_b32 v9, 7, v0\n"
        "v_mul_u32_u24 v1, s6, v4\n"
        "v_lshl_add_u32 v1, v9, 4, v1\n"
        "s_cmp_ge_u32 s7, 27\n"
        "s_waitcnt lgkmcnt(0)\n"
        "s_cselect_b32 s10, 0, s10\n"
        "s_memrealtime s[54:55]\n"
        "buffer_load_dwordx4 v[16:19], v1, s[8:11], s41 offen nt\n"
        "s_add_u32 s41, s41, s40\n"
        "buffer_load_dwordx4 v[20:23], v1, s[8:11], s41 offen nt\n"
        "s_add_u32 s41, s41, s53\n"
        "buffer_load_dwordx4 v[24:27], v1, s[8:11], s41 offen nt\n"
        "s_add_u32 s41, s41, s40\n"
        "buffer_load_dwordx4 v[28:31], v1, s[8:11], s41 offen nt\n"
        "s_add_u32 s41, s41, s53\n"
        "buffer_load_dwordx4 v[32:35], v1, s[8:11], s41 offen nt\n"
        "s_add_u32 s41, s41, s40\n"
        "buffer_load_dwordx4 v[36:39], v1, s[8:11], s41 offen nt\n"
        "s_add_u32 s41, s41, s53\n"
        "buffer_load_dwordx4 v[40:43], v1, s[8:11], s41 offen nt\n"
        "s_add_u32 s41, s41, s40\n"
        "buffer_load_dwordx4 v[44:47], v1, s[8:11], s41 offen nt\n"
        "s_add_u32 s41, s41, s53\n"
        "s_lshr_b32 s6, s5, 2\n"
        "s_lshl_b32 s6, s6, 5\n"
        "s_add_u32 s6, s6, s4\n"
        "s_lshl_b32 s46, s6, 20\n"
        "s_mul_i32 s6, s2, 0x9e3779b1\n"
        "s_lshr_b32 s6, s6, 14\n"
        "s_and_b32 s6, s6, 159744\n"
        "s_mul_i32 s7, s3, 9216\n"
        "s_xor_b32 s6, s6, s7\n"
        "s_and_b32 s7, s5, 3\n"
        "s_mul_i32 s7, s7, 32768\n"
        "s_xor_b32 s6, s6, s7\n"
        "s_or_b32 s46, s46, s6\n"
        "v_lshlrev_b32 v2, 4, v0\n"
        "s_mov_b32 s42, 0x05040100\n"
        "s_mov_b32 s43, 0x07060302\n"
        "s_mul_i32 s6, s3, 160\n"
        "v_lshl_add_u32 v3, v4, 2, s6\n"
        "v_and_b32 v5, 3, v4\n"
        "v_lshl_add_u32 v5, v9, 2, v5\n"
        "v_and_or_b32 v5, v0, 32, v5\n"
        "s_lshl_b32 s6, s3, 9\n"
        "v_lshl_add_u32 v5, v5, 3, s6\n"
        "s_lshl_b32 s6, s3, 3\n"
        "v_mov_b32 v6, s6\n"
        "v_mov_b32 v11, 0\n"
        "s_waitcnt lgkmcnt(0)\n"
        "s_waitcnt vmcnt(7)\n"
        "buffer_load_dwordx4 v[48:51], v1, s[8:11], s41 offen nt\n"
        "s_add_u32 s41, s41, s40\n"
        "s_waitcnt vmcnt(7)\n"
        "buffer_load_dwordx4 v[52:55], v1, s[8:11], s41 offen nt\n"
        "s_waitcnt vmcnt(7)\n"
        "s_addk_i32 s54, 125\n"
        ".Lfd_p10:\n"
        "s_memrealtime s[56:57]\n"
        "s_waitcnt lgkmcnt(0)\n"
        "s_sub_u32 s56, s56, s54\n"
        "s_cmp_lt_i32 s56, 0\n"
        "s_cbranch_scc1 .Lfd_p10\n"
        "buffer_load_dwordx4 v[64:67], v2, s[12:15], s46 offen nt\n"
        "s_xor_b32 s46, s46, 135168\n"
        "s_waitcnt vmcnt(7)\n"
        "buffer_load_dwordx4 v[68:71], v2, s[12:15], s46 offen nt\n"
        "s_xor_b32 s46, s46, 143360\n"
        "s_waitcnt vmcnt(7)\n"
        "s_addk_i32 s54, 29\n"
        ".Lfd_p12:\n"
        "s_memrealtime s[56:57]\n"
        "s_waitcnt lgkmcnt(0)\n"
        "s_sub_u32 s56, s56, s54\n"
        "s_cmp_lt_i32 s56, 0\n"
        "s_cbranch_scc1 .Lfd_p12\n"
        "buffer_load_dwordx4 v[72:75], v2, s[12:15], s46 offen nt\n"
        "s_xor_b32 s46, s46, 135168\n"
        "s_barrier\n"
        "ds_read2_b32 v[208:209], v3 offset0:0 offset1:8\n"
        "ds_read2_b32 v[212:213], v3 offset0:160 offset1:168\n"
        "ds_read2_b32 v[210:211], v3 offset0:16 offset1:24\n"
        "ds_read2_b32 v[214:215], v3 offset0:176 offset1:184\n"
        "ds_read2_b32 v[216:217], v3 offset0:32 offset1:192\n"
        "ds_read_b64 v[218:219], v11 offset:1280\n"
        "v_perm_b32 v220, v20, v16, s42\n"
        "v_perm_b32 v221, v20, v16, s43\n"
        "v_perm_b32 v222, v21, v17, s42\n"
        "v_perm_b32 v223, v21, v17, s43\n"
        "v_perm_b32 v224, v22, v18, s42\n"
        "v_perm_b32 v225, v22, v18, s43\n"
        "v_perm_b32 v226, v23, v19, s42\n"
        "v_perm_b32 v227, v23, v19, s43\n"
        "s_waitcnt lgkmcnt(0)\n"
        "v_readfirstlane_b32 s44, v218\n"
        "v_dot2_f32_bf16 v192, v220, v208, 0\n"
        "v_dot2_f32_bf16 v193, v221, v208, 0\n"
        "v_dot2_f32_bf16 v194, v222, v208, 0\n"
        "v_dot2_f32_bf16 v195, v223, v208, 0\n"
        "v_dot2_f32_bf16 v196, v224, v208, 0\n"
        "v_dot2_f32_bf16 v197, v225, v208, 0\n"
        "v_dot2_f32_bf16 v198, v226, v208, 0\n"
        "v_dot2_f32_bf16 v199, v227, v208, 0\n"
        "v_dot2_f32_bf16 v200, v220, v212, 0\n"
        "v_dot2_f32_bf16 v201, v221, v212, 0\n"
        "v_dot2_f32_bf16 v202, v222, v212, 0\n"
        "v_dot2_f32_bf16 v203, v223, v212, 0\n"
        "v_dot2_f32_bf16 v204, v224, v212, 0\n"
        "v_dot2_f32_bf16 v205, v225, v212, 0\n"
        "v_dot2_f32_bf16 v206, v226, v212, 0\n"
        "v_dot2_f32_bf16 v207, v227, v212, 0\n"
        "s_waitcnt vmcnt(7)\n"
        "buffer_load_dwordx4 v[76:79], v2, s[12:15], s46 offen nt\n"
        "s_xor_b32 s46, s46, 159744\n"
        "v_perm_b32 v220, v28, v24, s42\n"
        "v_perm_b32 v221, v28, v24, s43\n"
        "v_perm_b32 v222, v29, v25, s42\n"
        "v_perm_b32 v223, v29, v25, s43\n"
        "v_perm_b32 v224, v30, v26, s42\n"
        "v_perm_b32 v225, v30, v26, s43\n"
        "v_perm_b32 v226, v31, v27, s42\n"
        "v_perm_b32 v227, v31, v27, s43\n"
        "v_dot2c_f32_bf16 v192, v220, v209\n"
        "v_dot2c_f32_bf16 v193, v221, v209\n"
        "v_dot2c_f32_bf16 v194, v222, v209\n"
        "v_dot2c_f32_bf16 v195, v223, v209\n"
        "v_dot2c_f32_bf16 v196, v224, v209\n"
        "v_dot2c_f32_bf16 v197, v225, v209\n"
        "v_dot2c_f32_bf16 v198, v226, v209\n"
        "v_dot2c_f32_bf16 v199, v227, v209\n"
        "v_dot2c_f32_bf16 v200, v220, v213\n"
        "v_dot2c_f32_bf16 v201, v221, v213\n"
        "v_dot2c_f32_bf16 v202, v222, v213\n"
        "v_dot2c_f32_bf16 v203, v223, v213\n"
        "v_dot2c_f32_bf16 v204, v224, v213\n"
        "v_dot2c_f32_bf16 v205, v225, v213\n"
        "v_dot2c_f32_bf16 v206, v226, v213\n"
        "v_dot2c_f32_bf16 v207, v227, v213\n"
        "s_waitcnt vmcnt(7)\n"
        "s_addk_i32 s54, 29\n"
        ".Lfd_p14:\n"
        "s_memrealtime s[56:57]\n"
        "s_waitcnt lgkmcnt(0)\n"
        "s_sub_u32 s56, s56, s54\n"
        "s_cmp_lt_i32 s56, 0\n"
        "s_cbranch_scc1 .Lfd_p14\n"
        "buffer_load_dwordx4 v[80:83], v2, s[12:15], s46 offen nt\n"
        "s_xor_b32 s46, s46, 135168\n"
        "v_perm_b32 v220, v36, v32, s42\n"
        "v_perm_b32 v221, v36, v32, s43\n"
        "v_perm_b32 v222, v37, v33, s42\n"
        "v_perm_b32 v223, v37, v33, s43\n"
        "v_perm_b32 v224, v38, v34, s42\n"
        "v_perm_b32 v225, v38, v34, s43\n"
        "v_perm_b32 v226, v39, v35, s42\n"
        "v_perm_b32 v227, v39, v35, s43\n"
        "v_dot2c_f32_bf16 v192, v220, v210\n"
        "v_dot2c_f32_bf16 v193, v221, v210\n"
        "v_dot2c_f32_bf16 v194, v222, v210\n"
        "v_dot2c_f32_bf16 v195, v223, v210\n"
        "v_dot2c_f32_bf16 v196, v224, v210\n"
        "v_dot2c_f32_bf16 v197, v225, v210\n"
        "v_dot2c_f32_bf16 v198, v226, v210\n"
        "v_dot2c_f32_bf16 v199, v227, v210\n"
        "v_dot2c_f32_bf16 v200, v220, v214\n"
        "v_dot2c_f32_bf16 v201, v221, v214\n"
        "v_dot2c_f32_bf16 v202, v222, v214\n"
        "v_dot2c_f32_bf16 v203, v223, v214\n"
        "v_dot2c_f32_bf16 v204, v224, v214\n"
        "v_dot2c_f32_bf16 v205, v225, v214\n"
        "v_dot2c_f32_bf16 v206, v226, v214\n"
        "v_dot2c_f32_bf16 v207, v227, v214\n"
        "s_waitcnt vmcnt(7)\n"
        "buffer_load_dwordx4 v[84:87], v2, s[12:15], s46 offen nt\n"
        "s_xor_b32 s46, s46, 143360\n"
        "v_perm_b32 v220, v44, v40, s42\n"
        "v_perm_b32 v221, v44, v40, s43\n"
        "v_perm_b32 v222, v45, v41, s42\n"
        "v_perm_b32 v223, v45, v41, s43\n"
        "v_perm_b32 v224, v46, v42, s42\n"
        "v_perm_b32 v225, v46, v42, s43\n"
        "v_perm_b32 v226, v47, v43, s42\n"
        "v_perm_b32 v227, v47, v43, s43\n"
        "v_dot2c_f32_bf16 v192, v220, v211\n"
        "v_dot2c_f32_bf16 v193, v221, v211\n"
        "v_dot2c_f32_bf16 v194, v222, v211\n"
        "v_dot2c_f32_bf16 v195, v223, v211\n"
        "v_dot2c_f32_bf16 v196, v224, v211\n"
        "v_dot2c_f32_bf16 v197, v225, v211\n"
        "v_dot2c_f32_bf16 v198, v226, v211\n"
        "v_dot2c_f32_bf16 v199, v227, v211\n"
        "v_dot2c_f32_bf16 v200, v220, v215\n"
        "v_dot2c_f32_bf16 v201, v221, v215\n"
        "v_dot2c_f32_bf16 v202, v222, v215\n"
        "v_dot2c_f32_bf16 v203, v223, v215\n"
        "v_dot2c_f32_bf16 v204, v224, v215\n"
        "v_dot2c_f32_bf16 v205, v225, v215\n"
        "v_dot2c_f32_bf16 v206, v226, v215\n"
        "v_dot2c_f32_bf16 v207, v227, v215\n"
        "s_waitcnt vmcnt(7)\n"
        "s_addk_i32 s54, 29\n"
        ".Lfd_p16:\n"
        "s_memrealtime s[56:57]\n"
        "s_waitcnt lgkmcnt(0)\n"
        "s_sub_u32 s56, s56, s54\n"
        "s_cmp_lt_i32 s56, 0\n"
        "s_cbranch_scc1 .Lfd_p16\n"
        "buffer_load_dwordx4 v[88:91], v2, s[12:15], s46 offen nt\n"
        "s_xor_b32 s46, s46, 135168\n"
        "s_waitcnt vmcnt(7)\n"
        "buffer_load_dwordx4 v[92:95], v2, s[12:15], s46 offen nt\n"
        "s_xor_b32 s46, s46, 28672\n"
        "v_perm_b32 v220, v52, v48, s42\n"
        "v_perm_b32 v221, v52, v48, s43\n"
        "v_perm_b32 v222, v53, v49, s42\n"
        "v_perm_b32 v223, v53, v49, s43\n"
        "v_perm_b32 v224, v54, v50, s42\n"
        "v_perm_b32 v225, v54, v50, s43\n"
        "v_perm_b32 v226, v55, v51, s42\n"
        "v_perm_b32 v227, v55, v51, s43\n"
        "v_dot2c_f32_bf16 v192, v220, v216\n"
        "v_dot2c_f32_bf16 v193, v221, v216\n"
        "v_dot2c_f32_bf16 v194, v222, v216\n"
        "v_dot2c_f32_bf16 v195, v223, v216\n"
        "v_dot2c_f32_bf16 v196, v224, v216\n"
        "v_dot2c_f32_bf16 v197, v225, v216\n"
        "v_dot2c_f32_bf16 v198, v226, v216\n"
        "v_dot2c_f32_bf16 v199, v227, v216\n"
        "v_dot2c_f32_bf16 v200, v220, v217\n"
        "v_dot2c_f32_bf16 v201, v221, v217\n"
        "v_dot2c_f32_bf16 v202, v222, v217\n"
        "v_dot2c_f32_bf16 v203, v223, v217\n"
        "v_dot2c_f32_bf16 v204, v224, v217\n"
        "v_dot2c_f32_bf16 v205, v225, v217\n"
        "v_dot2c_f32_bf16 v206, v226, v217\n"
        "v_dot2c_f32_bf16 v207, v227, v217\n"
        "v_permlane32_swap_b32 v192, v200\n"
        "v_permlane32_swap_b32 v193, v201\n"
        "v_permlane32_swap_b32 v194, v202\n"
        "v_permlane32_swap_b32 v195, v203\n"
        "v_permlane32_swap_b32 v196, v204\n"
        "v_permlane32_swap_b32 v197, v205\n"
        "v_permlane32_swap_b32 v198, v206\n"
        "v_permlane32_swap_b32 v199, v207\n"
        "v_add_f32 v192, v192, v200\n"
        "v_add_f32 v193, v193, v201\n"
        "v_add_f32 v194, v194, v202\n"
        "v_add_f32 v195, v195, v203\n"
        "v_add_f32 v196, v196, v204\n"
        "v_add_f32 v197, v197, v205\n"
        "v_add_f32 v198, v198, v206\n"
        "v_add_f32 v199, v199, v207\n"
        "v_permlane16_swap_b32 v192, v196\n"
        "v_permlane16_swap_b32 v193, v197\n"
        "v_permlane16_swap_b32 v194, v198\n"
        "v_permlane16_swap_b32 v195, v199\n"
        "v_add_f32 v192, v192, v196\n"
        "v_add_f32 v193, v193, v197\n"
        "v_add_f32 v194, v194, v198\n"
        "v_add_f32 v195, v195, v199\n"
        "v_add_f32_dpp v192, v192, v192 row_ror:8 row_mask:0xf bank_mask:0x3\n"
        "v_add_f32_dpp v193, v193, v193 row_ror:8 row_mask:0xf bank_mask:0x3\n"
        "s_nop 0\n"
        "v_add_f32_dpp v192, v194, v194 row_ror:8 row_mask:0xf bank_mask:0xc\n"
        "v_add_f32_dpp v193, v195, v195 row_ror:8 row_mask:0xf bank_mask:0xc\n"
        "ds_write_b64 v5, v[192:193] offset:2048\n"
        "s_waitcnt lgkmcnt(0)\n"
        "s_barrier\n"
        "s_waitcnt vmcnt(7)\n"
        "s_addk_i32 s54, 29\n"
        ".Lfd_p18:\n"
        "s_memrealtime s[56:57]\n"
        "s_waitcnt lgkmcnt(0)\n"
        "s_sub_u32 s56, s56, s54\n"
        "s_cmp_lt_i32 s56, 0\n"
        "s_cbranch_scc1 .Lfd_p18\n"
        "buffer_load_dwordx4 v[96:99], v2, s[12:15], s46 offen nt\n"
        "s_xor_b32 s46, s46, 135168\n"
        "s_waitcnt vmcnt(7)\n"
        "buffer_load_dwordx4 v[100:103], v2, s[12:15], s46 offen nt\n"
        "s_xor_b32 s46, s46, 143360\n"
        "s_lshr_b32 s6, s2, 3\n"
        "s_mul_i32 s48, s6, 26\n"
        "s_lshr_b32 s48, s48, 8\n"
        "s_mul_i32 s49, s48, 10\n"
        "s_sub_u32 s49, s6, s49\n"
        "s_cmp_lt_u32 s48, 2\n"
        "s_cselect_b32 s6, 4, 3\n"
        "s_xor_b32 s6, s6, s2\n"
        "s_and_b32 s6, s6, 7\n"
        "s_lshl_b32 s48, s48, 3\n"
        "s_or_b32 s48, s48, s6\n"
        "s_and_b32 s7, s48, 8\n"
        "s_lshl_b32 s7, s7, 1\n"
        "s_or_b32 s6, s6, s7\n"
        "s_and_b32 s7, s3, 2\n"
        "s_lshl_b32 s7, s7, 2\n"
        "s_or_b32 s6, s6, s7\n"
        "s_and_b32 s53, s3, 1\n"
        "s_lshl_b32 s52, s6, 4\n"
        "s_or_b32 s52, s52, s53\n"
        "s_lshl_b32 s52, s52, 8\n"
        "s_add_u32 s52, s52, 196608\n"
        "s_lshl_b32 s6, s6, 1\n"
        "s_or_b32 s6, s6, s53\n"
        "s_sub_u32 s7, s48, 16\n"
        "s_lshl_b32 s7, s7, 2\n"
        "s_or_b32 s7, s7, s3\n"
        "s_cmp_lt_u32 s48, 16\n"
        "s_cselect_b64 s[32:33], s[32:33], s[36:37]\n"
        "s_cselect_b32 s34, s34, s38\n"
        "s_cselect_b32 s52, s52, 2.0\n"
        "s_cselect_b32 s6, s6, s7\n"
        "s_mul_i32 s6, s6, 81920\n"
        "s_lshl_b32 s7, s49, 7\n"
        "s_add_u32 s6, s6, s7\n"
        "v_and_b32 v14, 15, v0\n"
        "v_lshrrev_b32 v15, 4, v0\n"
        "v_and_b32 v63, 1, v4\n"
        "v_lshl_add_u32 v8, v9, 4, s6\n"
        "v_lshl_add_u32 v60, v63, 2, v15\n"
        "s_movk_i32 s6, 0x2800\n"
        "v_mad_u32_u24 v8, v60, s6, v8\n"
        "s_movk_i32 s41, 0x1400\n"
        "v_lshrrev_b32 v60, 2, v14\n"
        "v_lshl_add_u32 v13, v60, 9, s52\n"
        "v_bfe_i32 v61, v0, 2, 1\n"
        "v_xor_b32 v61, v61, v0\n"
        "v_and_b32 v61, 3, v61\n"
        "v_lshl_add_u32 v13, v61, 4, v13\n"
        "v_lshl_add_u32 v13, v15, 6, v13\n"
        "s_lshl_b32 s6, s3, 7\n"
        "v_lshl_add_u32 v7, v63, 9, s6\n"
        "v_and_b32 v60, 4, v0\n"
        "v_lshl_add_u32 v7, v60, 4, v7\n"
        "v_lshl_add_u32 v7, v15, 4, v7\n"
        "v_lshlrev_b32 v62, 4, v14\n"
        "v_lshl_add_u32 v5, v14, 2, v15\n"
        "s_lshl_b32 s6, s3, 9\n"
        "v_lshl_add_u32 v5, v5, 3, s6\n"
        "s_mul_i32 s6, s3, 2560\n"
        "s_addk_i32 s6, 5120\n"
        "v_mad_u32_u24 v59, v0, 40, s6\n"
        "s_lshr_b32 s6, s3, 1\n"
        "s_mul_i32 s6, s6, 1280\n"
        "s_add_u32 s7, s7, s6\n"
        "s_mul_i32 s58, s53, 160\n"
        "s_add_u32 s6, s6, s58\n"
        "s_addk_i32 s6, 5120\n"
        "v_mad_u32_u24 v60, v63, 24, v15\n"
        "v_mad_u32_u24 v56, v60, 40, s6\n"
        "v_lshl_add_u32 v56, v9, 2, v56\n"
        "s_lshl_b32 s53, s53, 6\n"
        "s_add_u32 s7, s7, s53\n"
        "v_lshl_add_u32 v10, v15, 4, s7\n"
        "v_lshl_add_u32 v10, v9, 1, v10\n"
        "s_lshl_b32 s6, s49, 2\n"
        "s_add_u32 s6, s6, s3\n"
        "s_lshl_b32 s6, s6, 9\n"
        "s_addk_i32 s6, 4096\n"
        "v_lshl_add_u32 v58, v15, 7, s6\n"
        "v_lshl_add_u32 v58, v9, 3, v58\n"
        "s_mov_b32 s80, 0xcccccccc\n"
        "s_mov_b32 s81, s80\n"
        "s_mov_b32 s82, 0xaaaaaaaa\n"
        "s_mov_b32 s83, s82\n"
        "s_mov_b32 s52, 0x00ff00ff\n"
        "s_mov_b32 s53, s52\n"
        "s_mov_b32 s58, 0\n"
        "s_mov_b32 s59, 0x41780000\n"
        "s_mov_b32 s45, 262144\n"
        "buffer_load_short_d16_hi v11, v10, s[24:27], 0 offen\n"
        "s_waitcnt vmcnt(8)\n"
        "s_addk_i32 s54, 29\n"
        ".Lfd_p20:\n"
        "s_memrealtime s[56:57]\n"
        "s_waitcnt lgkmcnt(0)\n"
        "s_sub_u32 s56, s56, s54\n"
        "s_cmp_lt_i32 s56, 0\n"
        "s_cbranch_scc1 .Lfd_p20\n"
        "buffer_load_dwordx4 v[104:107], v2, s[12:15], s46 offen nt\n"
        "s_xor_b32 s46, s46, 135168\n"
        "s_waitcnt vmcnt(8)\n"
        "buffer_load_dwordx4 v[108:111], v2, s[12:15], s46 offen nt\n"
        "s_xor_b32 s46, s46, 159744\n"
        "s_waitcnt vmcnt(8)\n"
        "s_addk_i32 s54, 29\n"
        ".Lfd_p22:\n"
        "s_memrealtime s[56:57]\n"
        "s_waitcnt lgkmcnt(0)\n"
        "s_sub_u32 s56, s56, s54\n"
        "s_cmp_lt_i32 s56, 0\n"
        "s_cbranch_scc1 .Lfd_p22\n"
        "buffer_load_dwordx4 v[112:115], v2, s[12:15], s46 offen nt\n"
        "s_xor_b32 s46, s46, 135168\n"
        "s_waitcnt vmcnt(8)\n"
        "buffer_load_dwordx4 v[116:119], v2, s[12:15], s46 offen nt\n"
        "s_xor_b32 s46, s46, 143360\n"
        "s_waitcnt vmcnt(8)\n"
        "s_addk_i32 s54, 29\n"
        ".Lfd_p24:\n"
        "s_memrealtime s[56:57]\n"
        "s_waitcnt lgkmcnt(0)\n"
        "s_sub_u32 s56, s56, s54\n"
        "s_cmp_lt_i32 s56, 0\n"
        "s_cbranch_scc1 .Lfd_p24\n"
        "buffer_load_dwordx4 v[120:123], v2, s[12:15], s46 offen nt\n"
        "s_xor_b32 s46, s46, 135168\n"
        "s_waitcnt vmcnt(8)\n"
        "buffer_load_dwordx4 v[124:127], v2, s[12:15], s46 offen nt\n"
        "s_xor_b32 s46, s46, 28672\n"
        "s_waitcnt vmcnt(8)\n"
        "s_addk_i32 s54, 29\n"
        ".Lfd_p26:\n"
        "s_memrealtime s[56:57]\n"
        "s_waitcnt lgkmcnt(0)\n"
        "s_sub_u32 s56, s56, s54\n"
        "s_cmp_lt_i32 s56, 0\n"
        "s_cbranch_scc1 .Lfd_p26\n"
        "buffer_load_dwordx4 v[128:131], v2, s[16:19], s46 offen nt\n"
        "s_xor_b32 s46, s46, 135168\n"
        "s_waitcnt vmcnt(8)\n"
        "buffer_load_dwordx4 v[132:135], v2, s[16:19], s46 offen nt\n"
        "s_xor_b32 s46, s46, 143360\n"
        "s_waitcnt vmcnt(7)\n"
        "s_addk_i32 s54, 29\n"
        ".Lfd_p28:\n"
        "s_memrealtime s[56:57]\n"
        "s_waitcnt lgkmcnt(0)\n"
        "s_sub_u32 s56, s56, s54\n"
        "s_cmp_lt_i32 s56, 0\n"
        "s_cbranch_scc1 .Lfd_p28\n"
        "buffer_load_dwordx4 v[136:139], v2, s[16:19], s46 offen nt\n"
        "s_xor_b32 s46, s46, 135168\n"
        "s_waitcnt vmcnt(7)\n"
        "buffer_load_dwordx4 v[140:143], v2, s[16:19], s46 offen nt\n"
        "s_xor_b32 s46, s46, 159744\n"
        "s_waitcnt vmcnt(7)\n"
        "s_addk_i32 s54, 29\n"
        ".Lfd_p30:\n"
        "s_memrealtime s[56:57]\n"
        "s_waitcnt lgkmcnt(0)\n"
        "s_sub_u32 s56, s56, s54\n"
        "s_cmp_lt_i32 s56, 0\n"
        "s_cbranch_scc1 .Lfd_p30\n"
        "buffer_load_dwordx4 v[144:147], v2, s[16:19], s46 offen nt\n"
        "s_xor_b32 s46, s46, 135168\n"
        "s_waitcnt vmcnt(7)\n"
        "buffer_load_dwordx4 v[148:151], v2, s[16:19], s46 offen nt\n"
        "s_xor_b32 s46, s46, 143360\n"
        "s_waitcnt vmcnt(7)\n"
        "s_addk_i32 s54, 29\n"
        ".Lfd_p32:\n"
        "s_memrealtime s[56:57]\n"
        "s_waitcnt lgkmcnt(0)\n"
        "s_sub_u32 s56, s56, s54\n"
        "s_cmp_lt_i32 s56, 0\n"
        "s_cbranch_scc1 .Lfd_p32\n"
        "buffer_load_dwordx4 v[152:155], v2, s[16:19], s46 offen nt\n"
        "s_xor_b32 s46, s46, 135168\n"
        "s_waitcnt vmcnt(7)\n"
        "buffer_load_dwordx4 v[156:159], v2, s[16:19], s46 offen nt\n"
        "s_xor_b32 s46, s46, 28672\n"
        "s_waitcnt vmcnt(7)\n"
        "s_addk_i32 s54, 29\n"
        ".Lfd_p34:\n"
        "s_memrealtime s[56:57]\n"
        "s_waitcnt lgkmcnt(0)\n"
        "s_sub_u32 s56, s56, s54\n"
        "s_cmp_lt_i32 s56, 0\n"
        "s_cbranch_scc1 .Lfd_p34\n"
        "buffer_load_dwordx4 v[160:163], v2, s[16:19], s46 offen nt\n"
        "s_xor_b32 s46, s46, 135168\n"
        "s_barrier\n"
        "ds_read_b128 v[208:211], v62 offset:1536\n"
        "s_waitcnt lgkmcnt(0)\n"
        "v_dot2_f32_bf16 v192, v64, v208, 0\n"
        "v_dot2_f32_bf16 v193, v68, v208, 0\n"
        "v_dot2_f32_bf16 v194, v72, v208, 0\n"
        "v_dot2_f32_bf16 v195, v76, v208, 0\n"
        "v_dot2_f32_bf16 v196, v80, v208, 0\n"
        "v_dot2_f32_bf16 v197, v84, v208, 0\n"
        "v_dot2_f32_bf16 v198, v88, v208, 0\n"
        "v_dot2_f32_bf16 v199, v92, v208, 0\n"
        "v_dot2c_f32_bf16 v192, v65, v209\n"
        "v_dot2c_f32_bf16 v193, v69, v209\n"
        "v_dot2c_f32_bf16 v194, v73, v209\n"
        "v_dot2c_f32_bf16 v195, v77, v209\n"
        "v_dot2c_f32_bf16 v196, v81, v209\n"
        "v_dot2c_f32_bf16 v197, v85, v209\n"
        "v_dot2c_f32_bf16 v198, v89, v209\n"
        "v_dot2c_f32_bf16 v199, v93, v209\n"
        "v_dot2c_f32_bf16 v192, v66, v210\n"
        "v_dot2c_f32_bf16 v193, v70, v210\n"
        "v_dot2c_f32_bf16 v194, v74, v210\n"
        "v_dot2c_f32_bf16 v195, v78, v210\n"
        "v_dot2c_f32_bf16 v196, v82, v210\n"
        "v_dot2c_f32_bf16 v197, v86, v210\n"
        "v_dot2c_f32_bf16 v198, v90, v210\n"
        "v_dot2c_f32_bf16 v199, v94, v210\n"
        "v_dot2c_f32_bf16 v192, v67, v211\n"
        "v_dot2c_f32_bf16 v193, v71, v211\n"
        "v_dot2c_f32_bf16 v194, v75, v211\n"
        "v_dot2c_f32_bf16 v195, v79, v211\n"
        "v_dot2c_f32_bf16 v196, v83, v211\n"
        "v_dot2c_f32_bf16 v197, v87, v211\n"
        "v_dot2c_f32_bf16 v198, v91, v211\n"
        "v_dot2c_f32_bf16 v199, v95, v211\n"
        "s_waitcnt vmcnt(7)\n"
        "buffer_load_dwordx4 v[164:167], v2, s[16:19], s46 offen nt\n"
        "s_xor_b32 s46, s46, 143360\n"
        "v_dot2_f32_bf16 v200, v96, v208, 0\n"
        "v_dot2_f32_bf16 v201, v100, v208, 0\n"
        "v_dot2_f32_bf16 v202, v104, v208, 0\n"
        "v_dot2_f32_bf16 v203, v108, v208, 0\n"
        "v_dot2_f32_bf16 v204, v112, v208, 0\n"
        "v_dot2_f32_bf16 v205, v116, v208, 0\n"
        "v_dot2_f32_bf16 v206, v120, v208, 0\n"
        "v_dot2_f32_bf16 v207, v124, v208, 0\n"
        "v_dot2c_f32_bf16 v200, v97, v209\n"
        "v_dot2c_f32_bf16 v201, v101, v209\n"
        "v_dot2c_f32_bf16 v202, v105, v209\n"
        "v_dot2c_f32_bf16 v203, v109, v209\n"
        "v_dot2c_f32_bf16 v204, v113, v209\n"
        "v_dot2c_f32_bf16 v205, v117, v209\n"
        "v_dot2c_f32_bf16 v206, v121, v209\n"
        "v_dot2c_f32_bf16 v207, v125, v209\n"
        "v_dot2c_f32_bf16 v200, v98, v210\n"
        "v_dot2c_f32_bf16 v201, v102, v210\n"
        "v_dot2c_f32_bf16 v202, v106, v210\n"
        "v_dot2c_f32_bf16 v203, v110, v210\n"
        "v_dot2c_f32_bf16 v204, v114, v210\n"
        "v_dot2c_f32_bf16 v205, v118, v210\n"
        "v_dot2c_f32_bf16 v206, v122, v210\n"
        "v_dot2c_f32_bf16 v207, v126, v210\n"
        "v_dot2c_f32_bf16 v200, v99, v211\n"
        "v_dot2c_f32_bf16 v201, v103, v211\n"
        "v_dot2c_f32_bf16 v202, v107, v211\n"
        "v_dot2c_f32_bf16 v203, v111, v211\n"
        "v_dot2c_f32_bf16 v204, v115, v211\n"
        "v_dot2c_f32_bf16 v205, v119, v211\n"
        "v_dot2c_f32_bf16 v206, v123, v211\n"
        "v_dot2c_f32_bf16 v207, v127, v211\n"
        "s_waitcnt vmcnt(7)\n"
        "s_addk_i32 s54, 29\n"
        ".Lfd_p36:\n"
        "s_memrealtime s[56:57]\n"
        "s_waitcnt lgkmcnt(0)\n"
        "s_sub_u32 s56, s56, s54\n"
        "s_cmp_lt_i32 s56, 0\n"
        "s_cbranch_scc1 .Lfd_p36\n"
        "buffer_load_dwordx4 v[168:171], v2, s[16:19], s46 offen nt\n"
        "s_xor_b32 s46, s46, 135168\n"
        "v_add_f32_dpp v192, v192, v192 row_ror:8 row_mask:0xf bank_mask:0x3\n"
        "v_add_f32_dpp v193, v193, v193 row_ror:8 row_mask:0xf bank_mask:0x3\n"
        "v_add_f32_dpp v194, v194, v194 row_ror:8 row_mask:0xf bank_mask:0x3\n"
        "v_add_f32_dpp v195, v195, v195 row_ror:8 row_mask:0xf bank_mask:0x3\n"
        "v_add_f32_dpp v196, v196, v196 row_ror:8 row_mask:0xf bank_mask:0x3\n"
        "v_add_f32_dpp v197, v197, v197 row_ror:8 row_mask:0xf bank_mask:0x3\n"
        "v_add_f32_dpp v198, v198, v198 row_ror:8 row_mask:0xf bank_mask:0x3\n"
        "v_add_f32_dpp v199, v199, v199 row_ror:8 row_mask:0xf bank_mask:0x3\n"
        "v_add_f32_dpp v192, v200, v200 row_ror:8 row_mask:0xf bank_mask:0xc\n"
        "v_add_f32_dpp v193, v201, v201 row_ror:8 row_mask:0xf bank_mask:0xc\n"
        "v_add_f32_dpp v194, v202, v202 row_ror:8 row_mask:0xf bank_mask:0xc\n"
        "v_add_f32_dpp v195, v203, v203 row_ror:8 row_mask:0xf bank_mask:0xc\n"
        "v_add_f32_dpp v196, v204, v204 row_ror:8 row_mask:0xf bank_mask:0xc\n"
        "v_add_f32_dpp v197, v205, v205 row_ror:8 row_mask:0xf bank_mask:0xc\n"
        "v_add_f32_dpp v198, v206, v206 row_ror:8 row_mask:0xf bank_mask:0xc\n"
        "v_add_f32_dpp v199, v207, v207 row_ror:8 row_mask:0xf bank_mask:0xc\n"
        "v_add_f32_dpp v192, v192, v192 row_half_mirror row_mask:0xf bank_mask:0x5\n"
        "v_add_f32_dpp v193, v193, v193 row_half_mirror row_mask:0xf bank_mask:0x5\n"
        "v_add_f32_dpp v194, v194, v194 row_half_mirror row_mask:0xf bank_mask:0x5\n"
        "v_add_f32_dpp v195, v195, v195 row_half_mirror row_mask:0xf bank_mask:0x5\n"
        "v_add_f32_dpp v192, v196, v196 row_half_mirror row_mask:0xf bank_mask:0xa\n"
        "v_add_f32_dpp v193, v197, v197 row_half_mirror row_mask:0xf bank_mask:0xa\n"
        "v_add_f32_dpp v194, v198, v198 row_half_mirror row_mask:0xf bank_mask:0xa\n"
        "v_add_f32_dpp v195, v199, v199 row_half_mirror row_mask:0xf bank_mask:0xa\n"
        "v_cndmask_b32 v220, v194, v192, s[80:81]\n"
        "v_cndmask_b32 v221, v195, v193, s[80:81]\n"
        "v_cndmask_b32 v222, v192, v194, s[80:81]\n"
        "v_cndmask_b32 v223, v193, v195, s[80:81]\n"
        "s_nop 0\n"
        "v_add_f32_dpp v192, v220, v222 quad_perm:[2,3,0,1] row_mask:0xf bank_mask:0xf\n"
        "v_add_f32_dpp v193, v221, v223 quad_perm:[2,3,0,1] row_mask:0xf bank_mask:0xf\n"
        "v_cndmask_b32 v220, v193, v192, s[82:83]\n"
        "v_cndmask_b32 v221, v192, v193, s[82:83]\n"
        "s_nop 1\n"
        "v_add_f32_dpp v192, v220, v221 quad_perm:[1,0,3,2] row_mask:0xf bank_mask:0xf\n"
        "s_waitcnt vmcnt(7)\n"
        "buffer_load_dwordx4 v[172:175], v2, s[16:19], s46 offen nt\n"
        "s_xor_b32 s46, s46, 159744\n"
        "v_cvt_pk_bf16_f32 v192, v192, v192\n"
        "v_lshlrev_b32 v192, 16, v192\n"
        "v_mul_f32 v192, 0x3db504f3, v192\n"
        "v_cvt_pk_bf16_f32 v192, v192, v192\n"
        "v_lshlrev_b32 v192, 16, v192\n"
        "v_mov_b32 v194, v192\n"
        "s_nop 1\n"
        "v_max_f32_dpp v194, v194, v194 quad_perm:[1,0,3,2] row_mask:0xf bank_mask:0xf\n"
        "s_nop 1\n"
        "v_max_f32_dpp v194, v194, v194 quad_perm:[2,3,0,1] row_mask:0xf bank_mask:0xf\n"
        "s_nop 1\n"
        "v_max_f32_dpp v194, v194, v194 row_half_mirror row_mask:0xf bank_mask:0xf\n"
        "s_nop 1\n"
        "v_max_f32_dpp v194, v194, v194 row_ror:8 row_mask:0xf bank_mask:0xf\n"
        "v_mov_b32 v196, v194\n"
        "s_nop 1\n"
        "v_permlane16_swap_b32 v194, v196\n"
        "v_max_f32 v194, v194, v196\n"
        "v_mov_b32 v196, v194\n"
        "s_nop 1\n"
        "v_permlane32_swap_b32 v194, v196\n"
        "v_max_f32 v194, v194, v196\n"
        "v_sub_f32 v192, v192, v194\n"
        "v_mul_f32 v192, 0x3fb8aa3b, v192\n"
        "v_exp_f32 v192, v192\n"
        "s_nop 1\n"
        "v_mov_b32_dpp v196, v192 quad_perm:[1,0,3,2] row_mask:0xf bank_mask:0xf\n"
        "v_cvt_pk_bf16_f32 v219, v192, v196\n"
        "v_lshlrev_b32 v195, 16, v219\n"
        "s_nop 1\n"
        "v_add_f32_dpp v195, v195, v195 quad_perm:[1,0,3,2] row_mask:0xf bank_mask:0xf\n"
        "s_nop 1\n"
        "v_add_f32_dpp v195, v195, v195 quad_perm:[2,3,0,1] row_mask:0xf bank_mask:0xf\n"
        "s_nop 1\n"
        "v_add_f32_dpp v195, v195, v195 row_half_mirror row_mask:0xf bank_mask:0xf\n"
        "s_nop 1\n"
        "v_add_f32_dpp v195, v195, v195 row_ror:8 row_mask:0xf bank_mask:0xf\n"
        "v_mov_b32 v196, v195\n"
        "s_nop 1\n"
        "v_permlane16_swap_b32 v195, v196\n"
        "v_add_f32 v195, v195, v196\n"
        "v_mov_b32 v196, v195\n"
        "s_nop 1\n"
        "v_permlane32_swap_b32 v195, v196\n"
        "v_add_f32 v195, v195, v196\n"
        "s_mov_b64 exec, 1\n"
        "ds_write_b64 v6, v[194:195] offset:1792\n"
        "s_mov_b64 exec, -1\n"
        "s_waitcnt lgkmcnt(0)\n"
        "s_barrier\n"
        "s_waitcnt vmcnt(7)\n"
        "s_addk_i32 s54, 29\n"
        ".Lfd_p38:\n"
        "s_memrealtime s[56:57]\n"
        "s_waitcnt lgkmcnt(0)\n"
        "s_sub_u32 s56, s56, s54\n"
        "s_cmp_lt_i32 s56, 0\n"
        "s_cbranch_scc1 .Lfd_p38\n"
        "buffer_load_dwordx4 v[176:179], v2, s[16:19], s46 offen nt\n"
        "s_xor_b32 s46, s46, 135168\n"
        "v_mov_b32_dpp v208, v219 row_newbcast:0 row_mask:0xf bank_mask:0xf\n"
        "v_perm_b32 v220, v132, v128, s42\n"
        "v_perm_b32 v221, v132, v128, s43\n"
        "v_perm_b32 v222, v133, v129, s42\n"
        "v_perm_b32 v223, v133, v129, s43\n"
        "v_perm_b32 v224, v134, v130, s42\n"
        "v_perm_b32 v225, v134, v130, s43\n"
        "v_perm_b32 v226, v135, v131, s42\n"
        "v_perm_b32 v227, v135, v131, s43\n"
        "v_dot2_f32_bf16 v192, v220, v208, 0\n"
        "v_dot2_f32_bf16 v193, v221, v208, 0\n"
        "v_dot2_f32_bf16 v194, v222, v208, 0\n"
        "v_dot2_f32_bf16 v195, v223, v208, 0\n"
        "v_dot2_f32_bf16 v196, v224, v208, 0\n"
        "v_dot2_f32_bf16 v197, v225, v208, 0\n"
        "v_dot2_f32_bf16 v198, v226, v208, 0\n"
        "v_dot2_f32_bf16 v199, v227, v208, 0\n"
        "v_mov_b32_dpp v208, v219 row_newbcast:2 row_mask:0xf bank_mask:0xf\n"
        "v_perm_b32 v220, v140, v136, s42\n"
        "v_perm_b32 v221, v140, v136, s43\n"
        "v_perm_b32 v222, v141, v137, s42\n"
        "v_perm_b32 v223, v141, v137, s43\n"
        "v_perm_b32 v224, v142, v138, s42\n"
        "v_perm_b32 v225, v142, v138, s43\n"
        "v_perm_b32 v226, v143, v139, s42\n"
        "v_perm_b32 v227, v143, v139, s43\n"
        "v_dot2c_f32_bf16 v192, v220, v208\n"
        "v_dot2c_f32_bf16 v193, v221, v208\n"
        "v_dot2c_f32_bf16 v194, v222, v208\n"
        "v_dot2c_f32_bf16 v195, v223, v208\n"
        "v_dot2c_f32_bf16 v196, v224, v208\n"
        "v_dot2c_f32_bf16 v197, v225, v208\n"
        "v_dot2c_f32_bf16 v198, v226, v208\n"
        "v_dot2c_f32_bf16 v199, v227, v208\n"
        "s_waitcnt vmcnt(7)\n"
        "buffer_load_dwordx4 v[180:183], v2, s[16:19], s46 offen nt\n"
        "s_xor_b32 s46, s46, 143360\n"
        "v_mov_b32_dpp v208, v219 row_newbcast:4 row_mask:0xf bank_mask:0xf\n"
        "v_perm_b32 v220, v148, v144, s42\n"
        "v_perm_b32 v221, v148, v144, s43\n"
        "v_perm_b32 v222, v149, v145, s42\n"
        "v_perm_b32 v223, v149, v145, s43\n"
        "v_perm_b32 v224, v150, v146, s42\n"
        "v_perm_b32 v225, v150, v146, s43\n"
        "v_perm_b32 v226, v151, v147, s42\n"
        "v_perm_b32 v227, v151, v147, s43\n"
        "v_dot2c_f32_bf16 v192, v220, v208\n"
        "v_dot2c_f32_bf16 v193, v221, v208\n"
        "v_dot2c_f32_bf16 v194, v222, v208\n"
        "v_dot2c_f32_bf16 v195, v223, v208\n"
        "v_dot2c_f32_bf16 v196, v224, v208\n"
        "v_dot2c_f32_bf16 v197, v225, v208\n"
        "v_dot2c_f32_bf16 v198, v226, v208\n"
        "v_dot2c_f32_bf16 v199, v227, v208\n"
        "v_mov_b32_dpp v208, v219 row_newbcast:6 row_mask:0xf bank_mask:0xf\n"
        "s_waitcnt vmcnt(6)\n"
        "v_perm_b32 v220, v156, v152, s42\n"
        "v_perm_b32 v221, v156, v152, s43\n"
        "v_perm_b32 v222, v157, v153, s42\n"
        "v_perm_b32 v223, v157, v153, s43\n"
        "v_perm_b32 v224, v158, v154, s42\n"
        "v_perm_b32 v225, v158, v154, s43\n"
        "v_perm_b32 v226, v159, v155, s42\n"
        "v_perm_b32 v227, v159, v155, s43\n"
        "v_dot2c_f32_bf16 v192, v220, v208\n"
        "v_dot2c_f32_bf16 v193, v221, v208\n"
        "v_dot2c_f32_bf16 v194, v222, v208\n"
        "v_dot2c_f32_bf16 v195, v223, v208\n"
        "v_dot2c_f32_bf16 v196, v224, v208\n"
        "v_dot2c_f32_bf16 v197, v225, v208\n"
        "v_dot2c_f32_bf16 v198, v226, v208\n"
        "v_dot2c_f32_bf16 v199, v227, v208\n"
        "s_addk_i32 s54, 29\n"
        ".Lfd_p40:\n"
        "s_memrealtime s[56:57]\n"
        "s_waitcnt lgkmcnt(0)\n"
        "s_sub_u32 s56, s56, s54\n"
        "s_cmp_lt_i32 s56, 0\n"
        "s_cbranch_scc1 .Lfd_p40\n"
        "buffer_load_dwordx4 v[184:187], v2, s[16:19], s46 offen nt\n"
        "s_xor_b32 s46, s46, 135168\n"
        "v_mov_b32_dpp v208, v219 row_newbcast:8 row_mask:0xf bank_mask:0xf\n"
        "s_waitcnt vmcnt(5)\n"
        "v_perm_b32 v220, v164, v160, s42\n"
        "v_perm_b32 v221, v164, v160, s43\n"
        "v_perm_b32 v222, v165, v161, s42\n"
        "v_perm_b32 v223, v165, v161, s43\n"
        "v_perm_b32 v224, v166, v162, s42\n"
        "v_perm_b32 v225, v166, v162, s43\n"
        "v_perm_b32 v226, v167, v163, s42\n"
        "v_perm_b32 v227, v167, v163, s43\n"
        "v_dot2c_f32_bf16 v192, v220, v208\n"
        "v_dot2c_f32_bf16 v193, v221, v208\n"
        "v_dot2c_f32_bf16 v194, v222, v208\n"
        "v_dot2c_f32_bf16 v195, v223, v208\n"
        "v_dot2c_f32_bf16 v196, v224, v208\n"
        "v_dot2c_f32_bf16 v197, v225, v208\n"
        "v_dot2c_f32_bf16 v198, v226, v208\n"
        "v_dot2c_f32_bf16 v199, v227, v208\n"
        "buffer_load_dwordx4 v[188:191], v2, s[16:19], s46 offen nt\n"
        "v_mov_b32_dpp v208, v219 row_newbcast:10 row_mask:0xf bank_mask:0xf\n"
        "s_waitcnt vmcnt(4)\n"
        "v_perm_b32 v220, v172, v168, s42\n"
        "v_perm_b32 v221, v172, v168, s43\n"
        "v_perm_b32 v222, v173, v169, s42\n"
        "v_perm_b32 v223, v173, v169, s43\n"
        "v_perm_b32 v224, v174, v170, s42\n"
        "v_perm_b32 v225, v174, v170, s43\n"
        "v_perm_b32 v226, v175, v171, s42\n"
        "v_perm_b32 v227, v175, v171, s43\n"
        "v_dot2c_f32_bf16 v192, v220, v208\n"
        "v_dot2c_f32_bf16 v193, v221, v208\n"
        "v_dot2c_f32_bf16 v194, v222, v208\n"
        "v_dot2c_f32_bf16 v195, v223, v208\n"
        "v_dot2c_f32_bf16 v196, v224, v208\n"
        "v_dot2c_f32_bf16 v197, v225, v208\n"
        "v_dot2c_f32_bf16 v198, v226, v208\n"
        "v_dot2c_f32_bf16 v199, v227, v208\n"
        "s_addk_i32 s54, 29\n"
        ".Lfd_p42:\n"
        "s_memrealtime s[56:57]\n"
        "s_waitcnt lgkmcnt(0)\n"
        "s_sub_u32 s56, s56, s54\n"
        "s_cmp_lt_i32 s56, 0\n"
        "s_cbranch_scc1 .Lfd_p42\n"
        "buffer_load_dwordx4 v[16:19], v8, s[32:35], 0 offen offset:0 nt\n"
        "buffer_load_dwordx4 v[20:23], v8, s[32:35], 0 offen offset:1280 nt\n"
        "v_mov_b32_dpp v208, v219 row_newbcast:12 row_mask:0xf bank_mask:0xf\n"
        "s_waitcnt vmcnt(4)\n"
        "v_perm_b32 v220, v180, v176, s42\n"
        "v_perm_b32 v221, v180, v176, s43\n"
        "v_perm_b32 v222, v181, v177, s42\n"
        "v_perm_b32 v223, v181, v177, s43\n"
        "v_perm_b32 v224, v182, v178, s42\n"
        "v_perm_b32 v225, v182, v178, s43\n"
        "v_perm_b32 v226, v183, v179, s42\n"
        "v_perm_b32 v227, v183, v179, s43\n"
        "v_dot2c_f32_bf16 v192, v220, v208\n"
        "v_dot2c_f32_bf16 v193, v221, v208\n"
        "v_dot2c_f32_bf16 v194, v222, v208\n"
        "v_dot2c_f32_bf16 v195, v223, v208\n"
        "v_dot2c_f32_bf16 v196, v224, v208\n"
        "v_dot2c_f32_bf16 v197, v225, v208\n"
        "v_dot2c_f32_bf16 v198, v226, v208\n"
        "v_dot2c_f32_bf16 v199, v227, v208\n"
        "s_addk_i32 s54, 20\n"
        ".Lfd_p44:\n"
        "s_memrealtime s[56:57]\n"
        "s_waitcnt lgkmcnt(0)\n"
        "s_sub_u32 s56, s56, s54\n"
        "s_cmp_lt_i32 s56, 0\n"
        "s_cbranch_scc1 .Lfd_p44\n"
        "buffer_load_dwordx4 v[24:27], v8, s[32:35], 0 offen offset:2560 nt\n"
        "buffer_load_dwordx4 v[28:31], v8, s[32:35], 0 offen offset:3840 nt\n"
        "v_mov_b32_dpp v208, v219 row_newbcast:14 row_mask:0xf bank_mask:0xf\n"
        "s_waitcnt vmcnt(4)\n"
        "v_perm_b32 v220, v188, v184, s42\n"
        "v_perm_b32 v221, v188, v184, s43\n"
        "v_perm_b32 v222, v189, v185, s42\n"
        "v_perm_b32 v223, v189, v185, s43\n"
        "v_perm_b32 v224, v190, v186, s42\n"
        "v_perm_b32 v225, v190, v186, s43\n"
        "v_perm_b32 v226, v191, v187, s42\n"
        "v_perm_b32 v227, v191, v187, s43\n"
        "v_dot2c_f32_bf16 v192, v220, v208\n"
        "v_dot2c_f32_bf16 v193, v221, v208\n"
        "v_dot2c_f32_bf16 v194, v222, v208\n"
        "v_dot2c_f32_bf16 v195, v223, v208\n"
        "v_dot2c_f32_bf16 v196, v224, v208\n"
        "v_dot2c_f32_bf16 v197, v225, v208\n"
        "v_dot2c_f32_bf16 v198, v226, v208\n"
        "v_dot2c_f32_bf16 v199, v227, v208\n"
        "v_permlane32_swap_b32 v192, v196\n"
        "v_permlane32_swap_b32 v193, v197\n"
        "v_permlane32_swap_b32 v194, v198\n"
        "v_permlane32_swap_b32 v195, v199\n"
        "v_add_f32 v192, v192, v196\n"
        "v_add_f32 v193, v193, v197\n"
        "v_add_f32 v194, v194, v198\n"
        "v_add_f32 v195, v195, v199\n"
        "s_nop 0\n"
        "v_permlane16_swap_b32 v192, v194\n"
        "v_permlane16_swap_b32 v193, v195\n"
        "v_add_f32 v192, v192, v194\n"
        "v_add_f32 v193, v193, v195\n"
        "ds_write_b64 v5, v[192:193] offset:2048\n"
        "s_waitcnt lgkmcnt(0)\n"
        "s_barrier\n"
        "s_addk_i32 s54, 20\n"
        ".Lfd_p46:\n"
        "s_memrealtime s[56:57]\n"
        "s_waitcnt lgkmcnt(0)\n"
        "s_sub_u32 s56, s56, s54\n"
        "s_cmp_lt_i32 s56, 0\n"
        "s_cbranch_scc1 .Lfd_p46\n"
        "buffer_load_dwordx4 v[32:35], v8, s[32:35], s41 offen offset:0 nt\n"
        "buffer_load_dwordx4 v[36:39], v8, s[32:35], s41 offen offset:1280 nt\n"
        "s_addk_i32 s54, 20\n"
        ".Lfd_p48:\n"
        "s_memrealtime s[56:57]\n"
        "s_waitcnt lgkmcnt(0)\n"
        "s_sub_u32 s56, s56, s54\n"
        "s_cmp_lt_i32 s56, 0\n"
        "s_cbranch_scc1 .Lfd_p48\n"
        "buffer_load_dwordx4 v[40:43], v8, s[32:35], s41 offen offset:2560 nt\n"
        "buffer_load_dwordx4 v[44:47], v8, s[32:35], s41 offen offset:3840 nt\n"
        "s_waitcnt vmcnt(6)\n"
        "v_perm_b32 v64, v20, v16, s42\n"
        "v_perm_b32 v68, v20, v16, s43\n"
        "v_perm_b32 v72, v21, v17, s42\n"
        "v_perm_b32 v76, v21, v17, s43\n"
        "v_perm_b32 v80, v22, v18, s42\n"
        "v_perm_b32 v84, v22, v18, s43\n"
        "v_perm_b32 v88, v23, v19, s42\n"
        "v_perm_b32 v92, v23, v19, s43\n"
        "s_waitcnt vmcnt(4)\n"
        "v_perm_b32 v65, v28, v24, s42\n"
        "v_perm_b32 v69, v28, v24, s43\n"
        "v_perm_b32 v73, v29, v25, s42\n"
        "v_perm_b32 v77, v29, v25, s43\n"
        "v_perm_b32 v81, v30, v26, s42\n"
        "v_perm_b32 v85, v30, v26, s43\n"
        "v_perm_b32 v89, v31, v27, s42\n"
        "v_perm_b32 v93, v31, v27, s43\n"
        "s_waitcnt vmcnt(2)\n"
        "buffer_load_dwordx4 v[100:103], v13, s[20:23], 0 offen sc1\n"
        "buffer_load_dwordx4 v[104:107], v13, s[20:23], 0 offen offset:2048 sc1\n"
        "v_perm_b32 v66, v36, v32, s42\n"
        "v_perm_b32 v70, v36, v32, s43\n"
        "v_perm_b32 v74, v37, v33, s42\n"
        "v_perm_b32 v78, v37, v33, s43\n"
        "v_perm_b32 v82, v38, v34, s42\n"
        "v_perm_b32 v86, v38, v34, s43\n"
        "v_perm_b32 v90, v39, v35, s42\n"
        "v_perm_b32 v94, v39, v35, s43\n"
        "s_waitcnt vmcnt(2)\n"
        "v_perm_b32 v67, v44, v40, s42\n"
        "v_perm_b32 v71, v44, v40, s43\n"
        "v_perm_b32 v75, v45, v41, s42\n"
        "v_perm_b32 v79, v45, v41, s43\n"
        "v_perm_b32 v83, v46, v42, s42\n"
        "v_perm_b32 v87, v46, v42, s43\n"
        "v_perm_b32 v91, v47, v43, s42\n"
        "v_perm_b32 v95, v47, v43, s43\n"
        "s_cmp_ge_u32 s48, 16\n"
        "s_cbranch_scc1 .Lfd_wd\n"
        ".Lfd_o:\n"
        "s_waitcnt vmcnt(0)\n"
        "v_perm_b32 v50, v101, v103, s43\n"
        "v_perm_b32 v51, v105, v107, s43\n"
        "v_perm_b32 v52, v103, v101, s42\n"
        "v_max_f32_dpp v116, v50, v50 row_ror:4 row_mask:0xf bank_mask:0xf\n"
        "v_max_f32_dpp v118, v51, v51 row_ror:4 row_mask:0xf bank_mask:0xf\n"
        "v_perm_b32 v53, v107, v105, s42\n"
        "v_max_f32_dpp v116, v116, v116 row_ror:8 row_mask:0xf bank_mask:0xf\n"
        "v_max_f32_dpp v118, v118, v118 row_ror:8 row_mask:0xf bank_mask:0xf\n"
        "v_bitop3_b32 v52, v52, v53, s44 bitop3:0x7e\n"
        "v_sub_f32 v116, v50, v116\n"
        "v_sub_f32 v118, v51, v118\n"
        "v_cmp_ne_u32 vcc, 0, v52\n"
        "v_exp_f32 v116, v116\n"
        "v_exp_f32 v118, v118\n"
        "v_lshlrev_b32 v120, 16, v100\n"
        "v_and_b32 v121, 0xffff0000, v100\n"
        "v_lshlrev_b32 v122, 16, v102\n"
        "v_and_b32 v123, 0xffff0000, v102\n"
        "v_lshlrev_b32 v124, 16, v104\n"
        "v_and_b32 v125, 0xffff0000, v104\n"
        "v_lshlrev_b32 v126, 16, v106\n"
        "v_and_b32 v127, 0xffff0000, v106\n"
        "s_cbranch_vccz .Lfd_oj\n"
        "buffer_load_dwordx4 v[100:103], v13, s[20:23], 0 offen sc1\n"
        "buffer_load_dwordx4 v[104:107], v13, s[20:23], 0 offen offset:2048 sc1\n"
        "s_sub_u32 s45, s45, 1\n"
        "s_cbranch_scc0 .Lfd_o\n"
        ".Lfd_oj:\n"
        "v_add_f32_dpp v50, v116, v116 row_ror:8 row_mask:0xf bank_mask:0x3\n"
        "v_pk_mul_f32 v[120:121], v[120:121], v[116:117] op_sel_hi:[1,0]\n"
        "v_pk_mul_f32 v[122:123], v[122:123], v[116:117] op_sel_hi:[1,0]\n"
        "v_add_f32_dpp v50, v118, v118 row_ror:8 row_mask:0xf bank_mask:0xc\n"
        "v_pk_mul_f32 v[124:125], v[124:125], v[118:119] op_sel_hi:[1,0]\n"
        "v_pk_mul_f32 v[126:127], v[126:127], v[118:119] op_sel_hi:[1,0]\n"
        "v_add_f32_dpp v50, v50, v50 row_half_mirror row_mask:0xf bank_mask:0xf\n"
        "v_add_f32_dpp v120, v120, v120 row_ror:8 row_mask:0xf bank_mask:0x3\n"
        "v_add_f32_dpp v121, v121, v121 row_ror:8 row_mask:0xf bank_mask:0x3\n"
        "v_add_f32_dpp v122, v122, v122 row_ror:8 row_mask:0xf bank_mask:0x3\n"
        "v_add_f32_dpp v123, v123, v123 row_ror:8 row_mask:0xf bank_mask:0x3\n"
        "v_rcp_f32 v50, v50\n"
        "v_add_f32_dpp v120, v124, v124 row_ror:8 row_mask:0xf bank_mask:0xc\n"
        "v_add_f32_dpp v121, v125, v125 row_ror:8 row_mask:0xf bank_mask:0xc\n"
        "v_add_f32_dpp v122, v126, v126 row_ror:8 row_mask:0xf bank_mask:0xc\n"
        "v_add_f32_dpp v123, v127, v127 row_ror:8 row_mask:0xf bank_mask:0xc\n"
        "v_add_f32_dpp v120, v120, v120 row_half_mirror row_mask:0xf bank_mask:0xf\n"
        "v_add_f32_dpp v121, v121, v121 row_half_mirror row_mask:0xf bank_mask:0xf\n"
        "v_add_f32_dpp v122, v122, v122 row_half_mirror row_mask:0xf bank_mask:0xf\n"
        "v_add_f32_dpp v123, v123, v123 row_half_mirror row_mask:0xf bank_mask:0xf\n"
        "v_pk_mul_f32 v[120:121], v[120:121], v[50:51] op_sel_hi:[1,0]\n"
        "v_pk_mul_f32 v[122:123], v[122:123], v[50:51] op_sel_hi:[1,0]\n"
        "v_cvt_pk_bf16_f32 v96, v120, v121\n"
        "v_cvt_pk_bf16_f32 v97, v122, v123\n"
        "s_nop 0\n"
        "v_mov_b32_dpp v98, v96 quad_perm:[1,0,3,2] row_mask:0xf bank_mask:0xf\n"
        "v_mov_b32_dpp v99, v97 quad_perm:[1,0,3,2] row_mask:0xf bank_mask:0xf\n"
        ".Lfd_mm:\n"
        "s_nop 1\n"
        "v_mfma_f32_16x16x32_bf16 v[128:131], v[96:99], v[64:67], 0\n"
        "v_mfma_f32_16x16x32_bf16 v[132:135], v[96:99], v[68:71], 0\n"
        "v_mfma_f32_16x16x32_bf16 v[136:139], v[96:99], v[72:75], 0\n"
        "v_mfma_f32_16x16x32_bf16 v[140:143], v[96:99], v[76:79], 0\n"
        "v_mfma_f32_16x16x32_bf16 v[144:147], v[96:99], v[80:83], 0\n"
        "v_mfma_f32_16x16x32_bf16 v[148:151], v[96:99], v[84:87], 0\n"
        "v_mfma_f32_16x16x32_bf16 v[152:155], v[96:99], v[88:91], 0\n"
        "v_mfma_f32_16x16x32_bf16 v[156:159], v[96:99], v[92:95], 0\n"
        "s_nop 1\n"
        "ds_write2_b32 v59, v128, v132 offset0:0 offset1:1\n"
        "s_nop 0\n"
        "ds_write2_b32 v59, v136, v140 offset0:2 offset1:3\n"
        "s_nop 0\n"
        "ds_write2_b32 v59, v144, v148 offset0:4 offset1:5\n"
        "s_nop 0\n"
        "ds_write2_b32 v59, v152, v156 offset0:6 offset1:7\n"
        "s_waitcnt lgkmcnt(0)\n"
        "s_barrier\n"
        "ds_read2st64_b32 v[160:161], v56 offset0:0 offset1:10\n"
        "ds_read2st64_b32 v[162:163], v56 offset0:20 offset1:30\n"
        "s_waitcnt lgkmcnt(0)\n"
        "v_pk_add_f32 v[160:161], v[160:161], v[162:163]\n"
        "v_add_f32 v160, v160, v161\n"
        "s_nop 1\n"
        "v_add_f32_dpp v160, v160, v160 row_ror:8 row_mask:0xf bank_mask:0xf\n"
        "s_mov_b64 exec, s[52:53]\n"
        "v_cvt_f64_f32 v[160:161], v160\n"
        "v_add_f64 v[160:161], v[160:161], s[58:59]\n"
        "v_lshlrev_b64 v[160:161], 12, v[160:161]\n"
        "v_or_b32 v162, 1, v160\n"
        "v_mov_b32 v163, v161\n"
        "buffer_atomic_add_x2 v[162:163], v58, s[20:23], 0 offen sc0 sc1\n"
        "s_or_b32 s6, s3, s49\n"
        "v_xor_b32 v161, 0x80000000, v161\n"
        "v_mov_b32 v165, s44\n"
        "s_waitcnt vmcnt(0)\n"
        "v_and_b32 v164, 0xfff, v162\n"
        "v_lshl_add_u64 v[160:161], v[160:161], 0, v[162:163]\n"
        "v_cmp_eq_u32 vcc, 22, v164\n"
        "v_cvt_f32_u32 v160, v160\n"
        "v_cvt_f32_i32 v161, v161\n"
        "v_fmac_f32 v11, 0x2b800000, v160\n"
        "v_fmac_f32 v11, 0x3b800000, v161\n"
        "s_mov_b64 exec, vcc\n"
        "v_cvt_pk_bf16_f32 v11, v11, v11\n"
        "s_cbranch_vccz .Lfd_sdone\n"
        "buffer_store_short v11, v10, s[28:31], 0 offen sc1\n"
        "s_cmp_lg_u32 s6, 0\n"
        "s_cbranch_scc1 .Lfd_sdone\n"
        "s_and_b64 exec, exec, 1\n"
        "s_cbranch_scc0 .Lfd_sdone\n"
        "buffer_store_dword v165, off, s[20:23], 0 sc1\n"
        ".Lfd_sdone:\n"
        "s_endpgm\n"
        ".Lfd_wd:\n"
        "s_cmp_ge_u32 s48, 23\n"
        "s_cbranch_scc1 .Lfd_sdone\n"
        "s_barrier\n"
        "ds_read_b128 v[96:99], v7 offset:4096\n"
        "s_waitcnt lgkmcnt(0)\n"
        "s_branch .Lfd_mm\n"
        ".Lfd_helper:\n"
        "s_mov_b32 s80, 0x3f803f80\n"
        "s_mov_b32 s81, 0xffff0000\n"
        "v_mul_u32_u24 v3, 0x280, v1\n"
        "v_lshl_add_u32 v21, v2, 4, v3\n"
        "v_lshl_add_u32 v22, v2, 2, v3\n"
        "v_mov_b32 v18, 0\n"
        "v_mov_b32 v19, 0\n"
        "v_mov_b32 v23, 0\n"
        "s_waitcnt vmcnt(1)\n"
        "v_dot2c_f32_bf16 v18, s80, v4\n"
        "v_dot2c_f32_bf16 v19, s80, v5\n"
        "v_dot2c_f32_bf16 v18, s80, v6\n"
        "v_dot2c_f32_bf16 v19, s80, v7\n"
        "v_dot2c_f32_bf16 v18, s80, v8\n"
        "v_dot2c_f32_bf16 v19, s80, v10\n"
        "v_dot2c_f32_bf16 v18, s80, v11\n"
        "v_dot2c_f32_bf16 v19, s80, v12\n"
        "v_dot2c_f32_bf16 v18, s80, v13\n"
        "v_dot2c_f32_bf16 v19, s80, v14\n"
        "v_lshlrev_b32 v24, 16, v4\n"
        "v_and_b32 v25, s81, v4\n"
        "v_lshlrev_b32 v26, 16, v5\n"
        "v_and_b32 v27, s81, v5\n"
        "v_add_f32 v18, v18, v19\n"
        "v_lshlrev_b32 v28, 16, v6\n"
        "v_and_b32 v29, s81, v6\n"
        "v_add_f32_dpp v18, v18, v18 quad_perm:[1,0,3,2] row_mask:0xf bank_mask:0xf\n"
        "v_lshlrev_b32 v30, 16, v7\n"
        "v_and_b32 v31, s81, v7\n"
        "v_add_f32_dpp v18, v18, v18 quad_perm:[2,3,0,1] row_mask:0xf bank_mask:0xf\n"
        "v_lshlrev_b32 v32, 16, v8\n"
        "v_and_b32 v33, s81, v8\n"
        "v_add_f32_dpp v18, v18, v18 row_half_mirror row_mask:0xf bank_mask:0xf\n"
        "v_lshlrev_b32 v34, 16, v10\n"
        "v_and_b32 v35, s81, v10\n"
        "v_add_f32_dpp v18, v18, v18 row_ror:8 row_mask:0xf bank_mask:0xf\n"
        "v_lshlrev_b32 v36, 16, v11\n"
        "v_and_b32 v37, s81, v11\n"
        "v_mov_b32 v19, v18\n"
        "v_lshlrev_b32 v38, 16, v12\n"
        "v_and_b32 v39, s81, v12\n"
        "v_permlane16_swap_b32 v18, v19\n"
        "v_lshlrev_b32 v40, 16, v13\n"
        "v_and_b32 v41, s81, v13\n"
        "v_add_f32 v18, v18, v19\n"
        "v_lshlrev_b32 v42, 16, v14\n"
        "v_and_b32 v43, s81, v14\n"
        "v_mul_f32 v18, 0x3acccccd, v18\n"
        "v_pk_add_f32 v[44:45], v[24:25], v[18:19] op_sel_hi:[1,0] neg_lo:[0,1] neg_hi:[0,1]\n"
        "v_pk_mul_f32 v[48:49], v[44:45], v[44:45]\n"
        "v_pk_add_f32 v[46:47], v[26:27], v[18:19] op_sel_hi:[1,0] neg_lo:[0,1] neg_hi:[0,1]\n"
        "v_pk_fma_f32 v[48:49], v[46:47], v[46:47], v[48:49]\n"
        "v_pk_add_f32 v[44:45], v[28:29], v[18:19] op_sel_hi:[1,0] neg_lo:[0,1] neg_hi:[0,1]\n"
        "v_pk_fma_f32 v[48:49], v[44:45], v[44:45], v[48:49]\n"
        "v_pk_add_f32 v[46:47], v[30:31], v[18:19] op_sel_hi:[1,0] neg_lo:[0,1] neg_hi:[0,1]\n"
        "v_pk_fma_f32 v[48:49], v[46:47], v[46:47], v[48:49]\n"
        "v_pk_add_f32 v[44:45], v[32:33], v[18:19] op_sel_hi:[1,0] neg_lo:[0,1] neg_hi:[0,1]\n"
        "v_pk_fma_f32 v[48:49], v[44:45], v[44:45], v[48:49]\n"
        "v_pk_add_f32 v[46:47], v[34:35], v[18:19] op_sel_hi:[1,0] neg_lo:[0,1] neg_hi:[0,1]\n"
        "v_pk_fma_f32 v[48:49], v[46:47], v[46:47], v[48:49]\n"
        "v_pk_add_f32 v[44:45], v[36:37], v[18:19] op_sel_hi:[1,0] neg_lo:[0,1] neg_hi:[0,1]\n"
        "v_pk_fma_f32 v[48:49], v[44:45], v[44:45], v[48:49]\n"
        "v_pk_add_f32 v[46:47], v[38:39], v[18:19] op_sel_hi:[1,0] neg_lo:[0,1] neg_hi:[0,1]\n"
        "v_pk_fma_f32 v[48:49], v[46:47], v[46:47], v[48:49]\n"
        "v_pk_add_f32 v[44:45], v[40:41], v[18:19] op_sel_hi:[1,0] neg_lo:[0,1] neg_hi:[0,1]\n"
        "v_pk_fma_f32 v[48:49], v[44:45], v[44:45], v[48:49]\n"
        "v_pk_add_f32 v[46:47], v[42:43], v[18:19] op_sel_hi:[1,0] neg_lo:[0,1] neg_hi:[0,1]\n"
        "v_pk_fma_f32 v[48:49], v[46:47], v[46:47], v[48:49]\n"
        "v_add_f32 v48, v48, v49\n"
        "v_cvt_pk_bf16_f32 v18, v18, v18\n"
        "v_lshlrev_b32 v18, 16, v18\n"
        "v_add_f32_dpp v48, v48, v48 quad_perm:[1,0,3,2] row_mask:0xf bank_mask:0xf\n"
        "v_pk_add_f32 v[24:25], v[24:25], v[18:19] op_sel_hi:[1,0] neg_lo:[0,1] neg_hi:[0,1]\n"
        "v_cvt_pk_bf16_f32 v4, v24, v25\n"
        "v_add_f32_dpp v48, v48, v48 quad_perm:[2,3,0,1] row_mask:0xf bank_mask:0xf\n"
        "v_pk_add_f32 v[26:27], v[26:27], v[18:19] op_sel_hi:[1,0] neg_lo:[0,1] neg_hi:[0,1]\n"
        "v_cvt_pk_bf16_f32 v5, v26, v27\n"
        "v_add_f32_dpp v48, v48, v48 row_half_mirror row_mask:0xf bank_mask:0xf\n"
        "v_pk_add_f32 v[28:29], v[28:29], v[18:19] op_sel_hi:[1,0] neg_lo:[0,1] neg_hi:[0,1]\n"
        "v_cvt_pk_bf16_f32 v6, v28, v29\n"
        "v_add_f32_dpp v48, v48, v48 row_ror:8 row_mask:0xf bank_mask:0xf\n"
        "v_pk_add_f32 v[30:31], v[30:31], v[18:19] op_sel_hi:[1,0] neg_lo:[0,1] neg_hi:[0,1]\n"
        "v_cvt_pk_bf16_f32 v7, v30, v31\n"
        "v_mov_b32 v49, v48\n"
        "v_pk_add_f32 v[32:33], v[32:33], v[18:19] op_sel_hi:[1,0] neg_lo:[0,1] neg_hi:[0,1]\n"
        "v_cvt_pk_bf16_f32 v8, v32, v33\n"
        "v_permlane16_swap_b32 v48, v49\n"
        "v_add_f32 v48, v48, v49\n"
        "v_mul_f32 v48, 0x3acccccd, v48\n"
        "v_cvt_pk_bf16_f32 v48, v48, v48\n"
        "v_lshlrev_b32 v48, 16, v48\n"
        "v_add_f32 v48, 0x3727c5ac, v48\n"
        "v_cvt_pk_bf16_f32 v48, v48, v48\n"
        "v_lshlrev_b32 v48, 16, v48\n"
        "v_sqrt_f32 v48, v48\n"
        "s_nop 0\n"
        "v_cvt_pk_bf16_f32 v48, v48, v48\n"
        "v_lshlrev_b32 v48, 16, v48\n"
        "v_rcp_f32 v48, v48\n"
        "v_lshlrev_b32 v24, 16, v4\n"
        "v_and_b32 v25, s81, v4\n"
        "v_lshlrev_b32 v26, 16, v5\n"
        "v_and_b32 v27, s81, v5\n"
        "v_lshlrev_b32 v28, 16, v6\n"
        "v_and_b32 v29, s81, v6\n"
        "v_lshlrev_b32 v30, 16, v7\n"
        "v_and_b32 v31, s81, v7\n"
        "v_lshlrev_b32 v32, 16, v8\n"
        "v_and_b32 v33, s81, v8\n"
        "v_pk_mul_f32 v[24:25], v[24:25], v[48:49] op_sel_hi:[1,0]\n"
        "v_pk_mul_f32 v[26:27], v[26:27], v[48:49] op_sel_hi:[1,0]\n"
        "v_pk_mul_f32 v[28:29], v[28:29], v[48:49] op_sel_hi:[1,0]\n"
        "v_pk_mul_f32 v[30:31], v[30:31], v[48:49] op_sel_hi:[1,0]\n"
        "v_pk_mul_f32 v[32:33], v[32:33], v[48:49] op_sel_hi:[1,0]\n"
        "v_cvt_pk_bf16_f32 v4, v24, v25\n"
        "v_cvt_pk_bf16_f32 v5, v26, v27\n"
        "v_cvt_pk_bf16_f32 v6, v28, v29\n"
        "v_cvt_pk_bf16_f32 v7, v30, v31\n"
        "v_cvt_pk_bf16_f32 v8, v32, v33\n"
        "ds_write_b128 v21, v[4:7] offset:0\n"
        "ds_write_b32 v22, v8 offset:512\n"
        "s_waitcnt vmcnt(0)\n"
        "v_add_u32 v16, 0x01010101, v16\n"
        "s_mov_b64 exec, 1\n"
        "ds_write_b32 v23, v16 offset:1280\n"
        "s_mov_b64 exec, -1\n"
        "v_readfirstlane_b32 s44, v16\n"
        "s_sub_u32 s83, s2, 240\n"
        "s_cmp_ge_u32 s83, 5\n"
        "s_cbranch_scc1 .Lfd_z0\n"
        "v_and_b32 v3, 3, v0\n"
        "v_lshrrev_b32 v20, 2, v0\n"
        "v_lshlrev_b32 v20, 7, v20\n"
        "v_lshl_add_u32 v20, v3, 4, v20\n"
        "v_mov_b32 v28, 0\n"
        "v_mov_b32 v29, 0\n"
        "v_mov_b32 v30, 0\n"
        "v_mov_b32 v31, 0\n"
        "s_lshl_b32 s6, s83, 12\n"
        "s_addk_i32 s6, 4096\n"
        "buffer_store_dwordx4 v[28:31], v20, s[20:23], s6 offen sc1\n"
        "s_addk_i32 s6, 2048\n"
        "buffer_store_dwordx4 v[28:31], v20, s[20:23], s6 offen sc1\n"
        ".Lfd_z0:\n"
        "s_waitcnt lgkmcnt(0)\n"
        "s_barrier\n"
        "s_lshr_b32 s6, s2, 3\n"
        "s_mul_i32 s48, s6, 26\n"
        "s_lshr_b32 s48, s48, 8\n"
        "s_mul_i32 s49, s48, 10\n"
        "s_sub_u32 s49, s6, s49\n"
        "s_cmp_lt_u32 s48, 2\n"
        "s_cselect_b32 s6, 4, 3\n"
        "s_xor_b32 s6, s6, s2\n"
        "s_and_b32 s6, s6, 7\n"
        "s_lshl_b32 s48, s48, 3\n"
        "s_or_b32 s48, s48, s6\n"
        "v_lshlrev_b32 v9, 4, v0\n"
        "v_and_b32 v14, 15, v0\n"
        "v_bfe_u32 v15, v0, 4, 1\n"
        "s_or_b32 s7, s50, s7\n"
        "s_lshl_b32 s51, s7, 2\n"
        "s_lshr_b32 s6, s5, 1\n"
        "s_or_b32 s51, s51, s6\n"
        "s_lshl_b32 s51, s51, 9\n"
        "s_add_u32 s51, s51, 65536\n"
        "s_cmp_lt_u32 s7, 60\n"
        "s_cselect_b32 s51, s51, 2.0\n"
        "s_lshl_b32 s6, s4, 3\n"
        "s_add_u32 s82, s6, s5\n"
        "s_lshl_b32 s82, s82, 9\n"
        "s_add_u32 s82, s82, 196608\n"
        "s_lshr_b32 s7, s5, 2\n"
        "s_add_u32 s7, s7, s6\n"
        "s_lshl_b32 s7, s7, 8\n"
        "s_add_u32 s7, s7, 65536\n"
        "v_lshrrev_b32 v4, 4, v0\n"
        "v_lshl_add_u32 v10, v4, 9, s7\n"
        "v_lshl_add_u32 v10, v14, 4, v10\n"
        "v_lshl_add_u32 v12, v14, 1, v15\n"
        "v_lshl_add_u32 v12, v1, 5, v12\n"
        "v_lshlrev_b32 v12, 2, v12\n"
        "s_lshl_b32 s52, s48, 13\n"
        "s_add_u32 s52, s52, 0\n"
        "s_add_u32 s53, s52, 0x1000\n"
        "s_and_b32 s56, s44, 0xff\n"
        "s_bfe_u32 s57, s44, 0x80008\n"
        "s_bfe_u32 s58, s44, 0x80010\n"
        "s_lshr_b32 s59, s44, 24\n"
        "v_mov_b32 v23, 0xffffff00\n"
        "v_add_u32 v5, s51, v9\n"
        "s_mov_b32 s45, 262144\n"
        "s_mov_b32 exec_hi, 0\n"
        "s_barrier\n"
        "ds_read_b128 v[28:31], v9 offset:2048\n"
        "ds_read_b128 v[32:35], v9 offset:2560\n"
        "ds_read_b128 v[36:39], v9 offset:3072\n"
        "ds_read_b128 v[40:43], v9 offset:3584\n"
        "s_waitcnt lgkmcnt(0)\n"
        "v_pk_add_f32 v[28:29], v[28:29], v[32:33]\n"
        "v_pk_add_f32 v[30:31], v[30:31], v[34:35]\n"
        "v_pk_add_f32 v[36:37], v[36:37], v[40:41]\n"
        "v_pk_add_f32 v[38:39], v[38:39], v[42:43]\n"
        "v_pk_add_f32 v[28:29], v[28:29], v[36:37]\n"
        "v_pk_add_f32 v[30:31], v[30:31], v[38:39]\n"
        "v_add_u32 v28, 0x80, v28\n"
        "v_add_u32 v29, 0x80, v29\n"
        "v_add_u32 v30, 0x80, v30\n"
        "v_add_u32 v31, 0x80, v31\n"
        "v_and_or_b32 v76, v28, v23, s56\n"
        "v_and_or_b32 v77, v29, v23, s57\n"
        "v_and_or_b32 v78, v30, v23, s58\n"
        "v_and_or_b32 v79, v31, v23, s59\n"
        "buffer_store_dwordx4 v[76:79], v5, s[20:23], 0 offen sc1\n"
        "s_cmp_ge_u32 s83, 5\n"
        "s_cbranch_scc1 .Lfd_z1\n"
        "s_lshl_b32 s6, s83, 7\n"
        "v_lshlrev_b32 v3, 2, v0\n"
        "s_waitcnt vmcnt(1)\n"
        "s_mov_b64 exec, 0xffff\n"
        "buffer_store_dword v16, v3, s[20:23], s6 offen offset:1024 sc1\n"
        ".Lfd_z1:\n"
        "s_mov_b64 exec, -1\n"
        "s_add_u32 s55, s54, 310\n"
        ".Lfd_qgate:\n"
        "s_sleep 2\n"
        "s_memrealtime s[6:7]\n"
        "s_waitcnt lgkmcnt(0)\n"
        "s_sub_u32 s6, s6, s55\n"
        "s_cmp_lt_i32 s6, 0\n"
        "s_cbranch_scc1 .Lfd_qgate\n"
        "buffer_load_dwordx4 v[80:83], v10, s[20:23], 0 offen sc1\n"
        "s_lshr_b32 s83, s49, 1\n"
        "s_lshl_b32 s83, s83, 7\n"
        "s_sub_u32 s47, s48, 16\n"
        "s_sleep 16\n"
        ".Lfd_q:\n"
        "buffer_load_dwordx4 v[84:87], v10, s[20:23], 0 offen sc1\n"
        "s_waitcnt vmcnt(1)\n"
        "v_xor_b32 v28, s56, v80\n"
        "v_xor_b32 v29, s57, v81\n"
        "v_xor_b32 v30, s58, v82\n"
        "v_xor_b32 v31, s59, v83\n"
        "v_or3_b32 v36, v28, v29, v30\n"
        "v_or_b32 v36, v36, v31\n"
        "v_and_b32 v36, 0xff, v36\n"
        "v_cmp_ne_u32 vcc, 0, v36\n"
        "s_cbranch_vccz .Lfd_q_ok\n"
        "buffer_load_dwordx4 v[80:83], v10, s[20:23], 0 offen sc1\n"
        "s_waitcnt vmcnt(1)\n"
        "v_xor_b32 v28, s56, v84\n"
        "v_xor_b32 v29, s57, v85\n"
        "v_xor_b32 v30, s58, v86\n"
        "v_xor_b32 v31, s59, v87\n"
        "v_or3_b32 v36, v28, v29, v30\n"
        "v_or_b32 v36, v36, v31\n"
        "v_and_b32 v36, 0xff, v36\n"
        "v_cmp_ne_u32 vcc, 0, v36\n"
        "s_cbranch_vccz .Lfd_q_ok\n"
        "s_sub_u32 s45, s45, 1\n"
        "s_cbranch_scc0 .Lfd_q\n"
        ".Lfd_q_ok:\n"
        "v_permlane16_swap_b32 v28, v30\n"
        "v_permlane16_swap_b32 v29, v31\n"
        "v_pk_add_f32 v[28:29], v[28:29], v[30:31]\n"
        "v_cvt_pk_bf16_f32 v28, v28, v29\n"
        "ds_write_b32 v12, v28 offset:1536\n"
        "buffer_load_dword v38, off, s[20:23], s83 offset:1024 sc1\n"
        "s_cmp_ge_u32 s47, 7\n"
        "s_cbranch_scc1 .Lfd_h1\n"
        "buffer_load_dwordx4 v[96:99], v9, s[20:23], s52 offen offset:0 sc1\n"
        "buffer_load_dwordx4 v[100:103], v9, s[20:23], s52 offen offset:1024 sc1\n"
        "buffer_load_dwordx4 v[104:107], v9, s[20:23], s52 offen offset:2048 sc1\n"
        "buffer_load_dwordx4 v[108:111], v9, s[20:23], s52 offen offset:3072 sc1\n"
        "buffer_load_dwordx4 v[112:115], v9, s[20:23], s53 offen offset:0 sc1\n"
        "buffer_load_dwordx4 v[116:119], v9, s[20:23], s53 offen offset:1024 sc1\n"
        "buffer_load_dwordx4 v[120:123], v9, s[20:23], s53 offen offset:2048 sc1\n"
        "buffer_load_dwordx4 v[124:127], v9, s[20:23], s53 offen offset:3072 sc1\n"
        ".Lfd_h1:\n"
        "s_waitcnt lgkmcnt(0)\n"
        "s_barrier\n"
        "v_and_b32 v3, 3, v0\n"
        "v_lshlrev_b32 v3, 3, v3\n"
        "v_bfe_u32 v4, v0, 1, 1\n"
        "v_xor_b32 v6, v4, v0\n"
        "v_and_b32 v6, 17, v6\n"
        "v_lshl_add_u32 v6, v4, 3, v6\n"
        "v_bfe_u32 v4, v0, 2, 2\n"
        "v_lshl_add_u32 v6, v4, 1, v6\n"
        "v_lshlrev_b32 v6, 4, v6\n"
        "s_mov_b32 s6, 0x07060100\n"
        "s_mov_b32 s7, 0x05040302\n"
        "s_mov_b32 exec_hi, 0\n"
        "s_barrier\n"
        "ds_read_b64 v[28:29], v3 offset:1792\n"
        "s_waitcnt lgkmcnt(0)\n"
        "v_max_f32_dpp v24, v28, v28 quad_perm:[1,0,3,2] row_mask:0xf bank_mask:0xf\n"
        "s_nop 1\n"
        "v_max_f32_dpp v24, v24, v24 quad_perm:[2,3,0,1] row_mask:0xf bank_mask:0xf\n"
        "v_sub_f32 v30, v28, v24\n"
        "v_mul_f32 v30, 0x3fb8aa3b, v30\n"
        "v_exp_f32 v30, v30\n"
        "s_nop 0\n"
        "v_mul_f32 v26, v30, v29\n"
        "s_nop 1\n"
        "v_add_f32_dpp v26, v26, v26 quad_perm:[1,0,3,2] row_mask:0xf bank_mask:0xf\n"
        "s_nop 1\n"
        "v_add_f32_dpp v26, v26, v26 quad_perm:[2,3,0,1] row_mask:0xf bank_mask:0xf\n"
        "v_rcp_f32 v29, v26\n"
        "v_log_f32 v26, v26\n"
        "v_mul_f32 v30, v30, v29\n"
        "v_fmac_f32 v26, 0x3fb8aa3b, v24\n"
        "v_readlane_b32 s8, v30, 0\n"
        "v_readlane_b32 s10, v30, 1\n"
        "v_readlane_b32 s12, v30, 2\n"
        "v_readlane_b32 s14, v30, 3\n"
        "v_perm_b32 v25, v26, v16, s6\n"
        "v_perm_b32 v27, v26, v16, s7\n"
        "s_barrier\n"
        "ds_read_b128 v[60:63], v6 offset:2048\n"
        "ds_read_b128 v[64:67], v6 offset:2560\n"
        "ds_read_b128 v[68:71], v6 offset:3072\n"
        "ds_read_b128 v[72:75], v6 offset:3584\n"
        "s_waitcnt lgkmcnt(3)\n"
        "v_pk_mul_f32 v[60:61], v[60:61], s[8:9] op_sel_hi:[1,0]\n"
        "v_pk_mul_f32 v[62:63], v[62:63], s[8:9] op_sel_hi:[1,0]\n"
        "s_waitcnt lgkmcnt(2)\n"
        "v_pk_fma_f32 v[60:61], v[64:65], s[10:11], v[60:61] op_sel_hi:[1,0,1]\n"
        "v_pk_fma_f32 v[62:63], v[66:67], s[10:11], v[62:63] op_sel_hi:[1,0,1]\n"
        "s_waitcnt lgkmcnt(1)\n"
        "v_pk_fma_f32 v[60:61], v[68:69], s[12:13], v[60:61] op_sel_hi:[1,0,1]\n"
        "v_pk_fma_f32 v[62:63], v[70:71], s[12:13], v[62:63] op_sel_hi:[1,0,1]\n"
        "s_waitcnt lgkmcnt(0)\n"
        "v_pk_fma_f32 v[60:61], v[72:73], s[14:15], v[60:61] op_sel_hi:[1,0,1]\n"
        "v_pk_fma_f32 v[62:63], v[74:75], s[14:15], v[62:63] op_sel_hi:[1,0,1]\n"
        "v_cvt_pk_bf16_f32 v24, v60, v61\n"
        "v_cvt_pk_bf16_f32 v26, v62, v63\n"
        "buffer_store_dwordx4 v[24:27], v9, s[20:23], s82 offen sc1\n"
        "s_mov_b64 exec, -1\n"
        "s_cmp_ge_u32 s48, 23\n"
        "s_cbranch_scc1 .Lfd_hdone\n"
        "s_mov_b32 s45, 262144\n"
        "s_waitcnt vmcnt(1)\n"
        ".Lfd_l:\n"
        "v_xor_b32 v37, v38, v16\n"
        "s_cmp_lt_u32 s48, 16\n"
        "s_cbranch_scc1 .Lfd_l1\n"
        "v_xor_b32 v96, s56, v96\n"
        "v_xor_b32 v97, s57, v97\n"
        "v_xor_b32 v98, s58, v98\n"
        "v_xor_b32 v99, s59, v99\n"
        "v_xor_b32 v100, s56, v100\n"
        "v_xor_b32 v101, s57, v101\n"
        "v_xor_b32 v102, s58, v102\n"
        "v_xor_b32 v103, s59, v103\n"
        "v_xor_b32 v104, s56, v104\n"
        "v_xor_b32 v105, s57, v105\n"
        "v_xor_b32 v106, s58, v106\n"
        "v_xor_b32 v107, s59, v107\n"
        "v_xor_b32 v108, s56, v108\n"
        "v_xor_b32 v109, s57, v109\n"
        "v_xor_b32 v110, s58, v110\n"
        "v_xor_b32 v111, s59, v111\n"
        "v_xor_b32 v112, s56, v112\n"
        "v_xor_b32 v113, s57, v113\n"
        "v_xor_b32 v114, s58, v114\n"
        "v_xor_b32 v115, s59, v115\n"
        "v_xor_b32 v116, s56, v116\n"
        "v_xor_b32 v117, s57, v117\n"
        "v_xor_b32 v118, s58, v118\n"
        "v_xor_b32 v119, s59, v119\n"
        "v_xor_b32 v120, s56, v120\n"
        "v_xor_b32 v121, s57, v121\n"
        "v_xor_b32 v122, s58, v122\n"
        "v_xor_b32 v123, s59, v123\n"
        "v_xor_b32 v124, s56, v124\n"
        "v_xor_b32 v125, s57, v125\n"
        "v_xor_b32 v126, s58, v126\n"
        "v_xor_b32 v127, s59, v127\n"
        "v_or3_b32 v36, v96, v97, v98\n"
        "v_or3_b32 v36, v36, v99, v100\n"
        "v_or3_b32 v36, v36, v101, v102\n"
        "v_or3_b32 v36, v36, v103, v104\n"
        "v_or3_b32 v36, v36, v105, v106\n"
        "v_or3_b32 v36, v36, v107, v108\n"
        "v_or3_b32 v36, v36, v109, v110\n"
        "v_or3_b32 v36, v36, v111, v112\n"
        "v_or3_b32 v36, v36, v113, v114\n"
        "v_or3_b32 v36, v36, v115, v116\n"
        "v_or3_b32 v36, v36, v117, v118\n"
        "v_or3_b32 v36, v36, v119, v120\n"
        "v_or3_b32 v36, v36, v121, v122\n"
        "v_or3_b32 v36, v36, v123, v124\n"
        "v_or3_b32 v36, v36, v125, v126\n"
        "v_or_b32 v36, v36, v127\n"
        "v_and_b32 v36, 0xff, v36\n"
        "v_or_b32 v37, v37, v36\n"
        ".Lfd_l1:\n"
        "v_cmp_ne_u32 vcc, 0, v37\n"
        "s_cbranch_vccnz .Lfd_l_retry\n"
        ".Lfd_l_ok:\n"
        "s_cmp_lt_u32 s48, 16\n"
        "s_cbranch_scc1 .Lfd_hlast\n"
        "v_lshl_add_u32 v3, v14, 1, v1\n"
        "v_lshl_add_u32 v3, v15, 7, v3\n"
        "v_lshlrev_b32 v3, 2, v3\n"
        "v_permlane32_swap_b32 v96, v98\n"
        "v_permlane32_swap_b32 v97, v99\n"
        "v_permlane32_swap_b32 v100, v102\n"
        "v_permlane32_swap_b32 v101, v103\n"
        "v_permlane32_swap_b32 v104, v106\n"
        "v_permlane32_swap_b32 v105, v107\n"
        "v_permlane32_swap_b32 v108, v110\n"
        "v_permlane32_swap_b32 v109, v111\n"
        "v_permlane32_swap_b32 v112, v114\n"
        "v_permlane32_swap_b32 v113, v115\n"
        "v_permlane32_swap_b32 v116, v118\n"
        "v_permlane32_swap_b32 v117, v119\n"
        "v_permlane32_swap_b32 v120, v122\n"
        "v_permlane32_swap_b32 v121, v123\n"
        "v_permlane32_swap_b32 v124, v126\n"
        "v_permlane32_swap_b32 v125, v127\n"
        "v_pk_add_f32 v[96:97], v[96:97], v[98:99]\n"
        "v_pk_add_f32 v[100:101], v[100:101], v[102:103]\n"
        "v_pk_add_f32 v[104:105], v[104:105], v[106:107]\n"
        "v_pk_add_f32 v[108:109], v[108:109], v[110:111]\n"
        "v_pk_add_f32 v[112:113], v[112:113], v[114:115]\n"
        "v_pk_add_f32 v[116:117], v[116:117], v[118:119]\n"
        "v_pk_add_f32 v[120:121], v[120:121], v[122:123]\n"
        "v_pk_add_f32 v[124:125], v[124:125], v[126:127]\n"
        "v_cvt_pk_bf16_f32 v96, v96, v97\n"
        "v_cvt_pk_bf16_f32 v100, v100, v101\n"
        "v_cvt_pk_bf16_f32 v104, v104, v105\n"
        "v_cvt_pk_bf16_f32 v108, v108, v109\n"
        "v_cvt_pk_bf16_f32 v112, v112, v113\n"
        "v_cvt_pk_bf16_f32 v116, v116, v117\n"
        "v_cvt_pk_bf16_f32 v120, v120, v121\n"
        "v_cvt_pk_bf16_f32 v124, v124, v125\n"
        "v_and_b32 v97, s81, v96\n"
        "v_lshlrev_b32 v96, 16, v96\n"
        "v_and_b32 v101, s81, v100\n"
        "v_lshlrev_b32 v100, 16, v100\n"
        "v_and_b32 v105, s81, v104\n"
        "v_lshlrev_b32 v104, 16, v104\n"
        "v_and_b32 v109, s81, v108\n"
        "v_lshlrev_b32 v108, 16, v108\n"
        "v_and_b32 v113, s81, v112\n"
        "v_lshlrev_b32 v112, 16, v112\n"
        "v_and_b32 v117, s81, v116\n"
        "v_lshlrev_b32 v116, 16, v116\n"
        "v_and_b32 v121, s81, v120\n"
        "v_lshlrev_b32 v120, 16, v120\n"
        "v_and_b32 v125, s81, v124\n"
        "v_lshlrev_b32 v124, 16, v124\n"
        "v_pk_mul_f32 v[98:99], v[96:97], v[96:97]\n"
        "v_mul_f32 v98, 0x3d372713, v98\n"
        "v_mul_f32 v99, 0x3d372713, v99\n"
        "v_pk_fma_f32 v[98:99], v[98:99], v[96:97], v[96:97]\n"
        "v_mul_f32 v98, 0xc0135761, v98\n"
        "v_mul_f32 v99, 0xc0135761, v99\n"
        "v_exp_f32 v98, v98\n"
        "v_exp_f32 v99, v99\n"
        "v_add_f32 v98, 1.0, v98\n"
        "v_add_f32 v99, 1.0, v99\n"
        "v_rcp_f32 v98, v98\n"
        "v_rcp_f32 v99, v99\n"
        "s_nop 0\n"
        "v_pk_mul_f32 v[96:97], v[96:97], v[98:99]\n"
        "v_cvt_pk_bf16_f32 v96, v96, v97\n"
        "v_and_b32 v97, s81, v96\n"
        "v_lshlrev_b32 v96, 16, v96\n"
        "v_pk_mul_f32 v[96:97], v[96:97], v[100:101]\n"
        "v_cvt_pk_bf16_f32 v96, v96, v97\n"
        "ds_write_b32 v3, v96 offset:4096\n"
        "v_pk_mul_f32 v[106:107], v[104:105], v[104:105]\n"
        "v_mul_f32 v106, 0x3d372713, v106\n"
        "v_mul_f32 v107, 0x3d372713, v107\n"
        "v_pk_fma_f32 v[106:107], v[106:107], v[104:105], v[104:105]\n"
        "v_mul_f32 v106, 0xc0135761, v106\n"
        "v_mul_f32 v107, 0xc0135761, v107\n"
        "v_exp_f32 v106, v106\n"
        "v_exp_f32 v107, v107\n"
        "v_add_f32 v106, 1.0, v106\n"
        "v_add_f32 v107, 1.0, v107\n"
        "v_rcp_f32 v106, v106\n"
        "v_rcp_f32 v107, v107\n"
        "s_nop 0\n"
        "v_pk_mul_f32 v[104:105], v[104:105], v[106:107]\n"
        "v_cvt_pk_bf16_f32 v104, v104, v105\n"
        "v_and_b32 v105, s81, v104\n"
        "v_lshlrev_b32 v104, 16, v104\n"
        "v_pk_mul_f32 v[104:105], v[104:105], v[108:109]\n"
        "v_cvt_pk_bf16_f32 v104, v104, v105\n"
        "ds_write_b32 v3, v104 offset:4224\n"
        "v_pk_mul_f32 v[114:115], v[112:113], v[112:113]\n"
        "v_mul_f32 v114, 0x3d372713, v114\n"
        "v_mul_f32 v115, 0x3d372713, v115\n"
        "v_pk_fma_f32 v[114:115], v[114:115], v[112:113], v[112:113]\n"
        "v_mul_f32 v114, 0xc0135761, v114\n"
        "v_mul_f32 v115, 0xc0135761, v115\n"
        "v_exp_f32 v114, v114\n"
        "v_exp_f32 v115, v115\n"
        "v_add_f32 v114, 1.0, v114\n"
        "v_add_f32 v115, 1.0, v115\n"
        "v_rcp_f32 v114, v114\n"
        "v_rcp_f32 v115, v115\n"
        "s_nop 0\n"
        "v_pk_mul_f32 v[112:113], v[112:113], v[114:115]\n"
        "v_cvt_pk_bf16_f32 v112, v112, v113\n"
        "v_and_b32 v113, s81, v112\n"
        "v_lshlrev_b32 v112, 16, v112\n"
        "v_pk_mul_f32 v[112:113], v[112:113], v[116:117]\n"
        "v_cvt_pk_bf16_f32 v112, v112, v113\n"
        "ds_write_b32 v3, v112 offset:4352\n"
        "v_pk_mul_f32 v[122:123], v[120:121], v[120:121]\n"
        "v_mul_f32 v122, 0x3d372713, v122\n"
        "v_mul_f32 v123, 0x3d372713, v123\n"
        "v_pk_fma_f32 v[122:123], v[122:123], v[120:121], v[120:121]\n"
        "v_mul_f32 v122, 0xc0135761, v122\n"
        "v_mul_f32 v123, 0xc0135761, v123\n"
        "v_exp_f32 v122, v122\n"
        "v_exp_f32 v123, v123\n"
        "v_add_f32 v122, 1.0, v122\n"
        "v_add_f32 v123, 1.0, v123\n"
        "v_rcp_f32 v122, v122\n"
        "v_rcp_f32 v123, v123\n"
        "s_nop 0\n"
        "v_pk_mul_f32 v[120:121], v[120:121], v[122:123]\n"
        "v_cvt_pk_bf16_f32 v120, v120, v121\n"
        "v_and_b32 v121, s81, v120\n"
        "v_lshlrev_b32 v120, 16, v120\n"
        "v_pk_mul_f32 v[120:121], v[120:121], v[124:125]\n"
        "v_cvt_pk_bf16_f32 v120, v120, v121\n"
        "ds_write_b32 v3, v120 offset:4480\n"
        "s_waitcnt lgkmcnt(0)\n"
        "s_barrier\n"
        ".Lfd_hlast:\n"
        "s_barrier\n"
        ".Lfd_hdone:\n"
        "s_endpgm\n"
        ".Lfd_l_retry:\n"
        "s_sub_u32 s45, s45, 1\n"
        "s_cbranch_scc1 .Lfd_l_ok\n"
        "buffer_load_dword v38, off, s[20:23], s83 offset:1024 sc1\n"
        "s_cmp_ge_u32 s47, 7\n"
        "s_cbranch_scc1 .Lfd_l2\n"
        "buffer_load_dwordx4 v[96:99], v9, s[20:23], s52 offen offset:0 sc1\n"
        "buffer_load_dwordx4 v[100:103], v9, s[20:23], s52 offen offset:1024 sc1\n"
        "buffer_load_dwordx4 v[104:107], v9, s[20:23], s52 offen offset:2048 sc1\n"
        "buffer_load_dwordx4 v[108:111], v9, s[20:23], s52 offen offset:3072 sc1\n"
        "buffer_load_dwordx4 v[112:115], v9, s[20:23], s53 offen offset:0 sc1\n"
        "buffer_load_dwordx4 v[116:119], v9, s[20:23], s53 offen offset:1024 sc1\n"
        "buffer_load_dwordx4 v[120:123], v9, s[20:23], s53 offen offset:2048 sc1\n"
        "buffer_load_dwordx4 v[124:127], v9, s[20:23], s53 offen offset:3072 sc1\n"
        ".Lfd_l2:\n"
        "s_waitcnt vmcnt(0)\n"
        "s_branch .Lfd_l\n"
        ".Lfd_end:\n"
        :
        : "{s[0:1]}"(__builtin_amdgcn_kernarg_segment_ptr()), "{s2}"(__builtin_amdgcn_workgroup_id_x()),
          "{v0}"(__builtin_amdgcn_workitem_id_x()), "{s3}"((unsigned)(size_t)(__attribute__((address_space(3))) unsigned*)smem)
        : "v227", "s83", "vcc", "memory");
}

extern "C" void kernel_launch(void* const* d_in, const int* in_sizes, int n_in, void* d_out, int out_size, void* d_ws, size_t ws_size,
                              hipStream_t stream) {
    const char* wq = (const char*)d_in[3];
    const unsigned WQ = 640u * 8192u, WG = 640u * 3456u, WIN = 3072u * 256u, KV = 64u * 4096u * 256u - WIN;
    fused_decode<<<256, 320, 0, stream>>>(wq, WQ, RAW, wq + 128, WQ - 128u, RAW, d_in[6], WG, RAW, d_in[7], WG, RAW,
                                          (const char*)d_in[1] + WIN, KV, RAW, (const char*)d_in[2] + WIN, KV, RAW,
                                          d_ws, 327680u, RAW, d_in[0], 2560u, RAW, d_out, 2560u, RAW,
                                          d_in[4], WQ, RAW, d_in[8], WG, RAW);
}
```
